```python
import torch

def _softmax(x, axis=-1):
    return torch.softmax(x, dim=axis)
def _gelu(x):
    return torch.nn.functional.gelu(x, approximate="tanh")
def _silu(x):
    return torch.nn.functional.silu(x)
def _sigmoid(x):
    return torch.sigmoid(x)
def _layernorm(x, axis=-1, eps=1e-5):
    m = x.mean(dim=axis, keepdim=True)
    v = x.var(dim=axis, keepdim=True, unbiased=False)
    return (x - m) / torch.sqrt(v + eps)
def _scatter_add(tgt, idx, src, axis=0):
    return tgt.clone().index_add_(axis, idx, src)
def _segment_sum(src, seg, n):
    out = torch.zeros((n,) + src.shape[1:], dtype=src.dtype)
    return out.index_add_(0, seg, src)
def _pad(x, pw):
    flat = tuple(v for pair in reversed(pw) for v in pair)
    return torch.nn.functional.pad(x, flat)

kernel_name = 'gated_mix__mha_block__moe_arch_mod__prompt'

def setup_inputs():

    _w = (lambda a: (lambda t: t.to(torch.bfloat16) if t.is_floating_point() else t)(torch.as_tensor(a)))
    def _A_setup():
        B, S, D, H = 1, 2048, 512, 32
        return {
            "x": _w((lambda *s: torch.randn(*s, dtype=torch.bfloat16) * 0.05)(B, S, D)),
            "wqkv": _w((lambda *s: torch.randn(*s, dtype=torch.bfloat16) * 0.8)(D, 3 * D)),
            "wo": _w((lambda *s: torch.randn(*s, dtype=torch.bfloat16) * 0.1)(D, D)),
        }
    _A_in = _A_setup()

    def _B_setup():
        B, S, D, F, C = 1, 2048, 512, 1792, 1
        return {
            "x": _w((lambda *s: torch.randn(*s, dtype=torch.bfloat16) * 0.05)(B, S, D)),
            "w_r": _w((lambda *s: torch.randn(*s, dtype=torch.bfloat16) * 1.6)(D)),
            "w_gate": _w((lambda *s: torch.randn(*s, dtype=torch.bfloat16) * 0.2)(D, F)),
            "w_up": _w((lambda *s: torch.randn(*s, dtype=torch.bfloat16) * 0.2)(D, F)),
            "w_down": _w((lambda *s: torch.randn(*s, dtype=torch.bfloat16) * 0.2)(F, D)),
        }
    _B_in = _B_setup()

    return {
        "A_x": _A_in["x"],
        "A_wqkv": _A_in["wqkv"],
        "A_wo": _A_in["wo"],
        "B_x": _B_in["x"],
        "B_w_r": _B_in["w_r"],
        "B_w_gate": _B_in["w_gate"],
        "B_w_up": _B_in["w_up"],
        "B_w_down": _B_in["w_down"],
        "gate_w": _w((lambda *s: torch.randn(*s, dtype=torch.bfloat16) * 0.05)(512) * 16.0),
    }

def reference(A_x, A_wqkv, A_wo, B_x, B_w_r, B_w_gate, B_w_up, B_w_down, gate_w):
    def _A_ref(x, wqkv, wo):
        B, S, D = x.shape
        H = 32
        Dh = D // H
        qkv = (lambda x, *d: x.permute(*d))((x @ wqkv).reshape(B, S, 3, H, Dh), 2, 0, 3, 1, 4)
        q, k, v = qkv[0], qkv[1], qkv[2]
        scores = torch.einsum("bhqd,bhkd->bhqk", q, k) * (Dh ** -0.5)
        p = _softmax(scores, axis=-1)
        o = torch.einsum("bhqk,bhkd->bhqd", p, v)
        return (lambda x, *d: x.permute(*d))(o, 0, 2, 1, 3).reshape(B, S, D) @ wo

    def _B_ref(x, w_r, w_gate, w_up, w_down):
        B, S, D = x.shape
        C = 1
        r = x @ w_r
        idx = torch.argsort(-r, axis=-1)[:, :C]
        idx_e = (lambda x, n, axis: torch.repeat_interleave(x, n, dim=axis))(idx.reshape(B, C, 1), D, axis=2)
        x_sel = (lambda x, i, axis: torch.take_along_dim(x, i, dim=axis))(x, idx_e, axis=1)
        g = _silu(x_sel @ w_gate)
        u = x_sel @ w_up
        y = (g * u) @ w_down
        r_sel = _sigmoid((lambda x, i, axis: torch.take_along_dim(x, i, dim=axis))(r, idx, axis=1))
        y = y * r_sel.reshape(B, C, 1)
        b_off = (torch.arange(B) * S).reshape(B, 1)
        flat_idx = (idx + b_off).reshape(B * C)
        zero = (x * 0.0).reshape(B * S, D)
        upd = _scatter_add(zero, flat_idx, y.reshape(B * C, D), axis=0)
        return x + upd.reshape(B, S, D)

    x = A_x
    g = _sigmoid((x * gate_w).sum(-1, keepdims=True))
    a = _A_ref(x, A_wqkv, A_wo)
    b = _B_ref(x, B_w_r, B_w_gate, B_w_up, B_w_down)
    return g * a + (1 - g) * b
```

```cpp
#include <hip/hip_runtime.h>
#include <hip/hip_bf16.h>
#include <stdint.h>

#define FUSED 0

typedef __attribute__((ext_vector_type(8))) __bf16 bf16x8;
typedef __attribute__((ext_vector_type(16))) __bf16 bf16x16;
typedef __attribute__((ext_vector_type(4))) float f32x4;
typedef __attribute__((ext_vector_type(8))) float f32x8;
typedef __attribute__((ext_vector_type(16))) float f32x16;
typedef __attribute__((ext_vector_type(4))) unsigned u32x4;
typedef __attribute__((ext_vector_type(2))) unsigned u32x2;
typedef __attribute__((ext_vector_type(4))) short s16x4;
typedef unsigned long long u64;
#define LAS __attribute__((address_space(3)))
#define GAS __attribute__((address_space(1)))
#define RLX_AGENT __ATOMIC_RELAXED, __HIP_MEMORY_SCOPE_AGENT

constexpr size_t WS_KV = 0;
constexpr size_t WS_O = 4u << 20;
constexpr size_t WS_YP = 6u << 20;
constexpr size_t WS_G = WS_YP + (512u << 10);
constexpr size_t WS_RG = WS_G + 8192;
constexpr size_t WS_KVF = WS_RG + 2048;
constexpr size_t WS_OF = WS_KVF + 16384;
constexpr size_t WS_YF = WS_OF + 16384;
constexpr size_t WS_EP = WS_YF + 2048;
constexpr size_t WS_Q = 8u << 20;

constexpr int SLOT = 28672, NSLOT = 5;
constexpr int L_MISC = SLOT * NSLOT;
constexpr int L_RK = L_MISC;
constexpr int L_FS = L_MISC + 64;
constexpr int L_FH = L_FS + 512;
constexpr int L_YS = L_FH + 64;
constexpr int L_Q = L_MISC + 2048;
constexpr int LDS_BYTES = L_Q + 8192;
constexpr int L_WO = 0;
constexpr int L_CB = 32768;

struct Params {
    const uint16_t* x; const uint16_t* wqkv; const uint16_t* wo; const uint16_t* wr;
    const uint16_t* wg; const uint16_t* wu; const uint16_t* wd; const uint16_t* gw;
    uint16_t* out; char* ws;
};

__device__ __forceinline__ float bflo(unsigned u) { return __uint_as_float(u << 16); }
__device__ __forceinline__ float bfhi(unsigned u) { return __uint_as_float(u & 0xffff0000u); }
__device__ __forceinline__ unsigned cvtpk(float lo, float hi) {
    typedef __attribute__((ext_vector_type(2))) float f2; typedef __attribute__((ext_vector_type(2))) __bf16 b2;
    return __builtin_bit_cast(unsigned, __builtin_convertvector((f2){lo, hi}, b2));
}
__device__ __forceinline__ float bfr(float v) { return bflo(cvtpk(v, 0.f) & 0xffffu); }
__device__ __forceinline__ float sigmoidf_(float v) { return 1.0f / (1.0f + __expf(-v)); }
__device__ __forceinline__ f32x4 mfma16(u32x4 a, u32x4 b, f32x4 c) {
    return __builtin_amdgcn_mfma_f32_16x16x32_bf16(__builtin_bit_cast(bf16x8, a), __builtin_bit_cast(bf16x8, b), c, 0, 0, 0);
}
__device__ __forceinline__ u32x2 trd(const LAS char* p) {
    return __builtin_bit_cast(u32x2, __builtin_amdgcn_ds_read_tr16_b64_v4i16((LAS s16x4*)p));
}
__device__ __forceinline__ u32x4 trd2(const LAS char* p, int d) { u32x2 a = trd(p), b = trd(p + d); return (u32x4){a[0], a[1], b[0], b[1]}; }
__device__ __forceinline__ void dma16(const void* sbase, unsigned voff, unsigned lds) {
    unsigned keep;
    asm volatile("s_mov_b32 %0, m0\n\ts_mov_b32 m0, %3\n\ts_nop 0\n\tglobal_load_lds_dwordx4 %1, %2\n\ts_mov_b32 m0, %0"
                 : "=&s"(keep) : "v"(voff), "s"(sbase), "s"(lds) : "memory");
}
__device__ __forceinline__ void dma16_half(const void* sbase, unsigned voff, unsigned lds) {
    unsigned keep;
    asm volatile("s_mov_b32 %0, m0\n\ts_mov_b32 m0, %3\n\ts_mov_b32 exec_hi, 0\n\tglobal_load_lds_dwordx4 %1, %2\n\ts_mov_b32 exec_hi, -1\n\ts_mov_b32 m0, %0"
                 : "=&s"(keep) : "v"(voff), "s"(sbase), "s"(lds) : "memory");
}
template <int N> __device__ __forceinline__ void wait_bar() { asm volatile("s_waitcnt vmcnt(%0) lgkmcnt(0)\n\ts_barrier" :: "n"(N) : "memory"); }
__device__ __forceinline__ void lds_bar() { asm volatile("s_waitcnt lgkmcnt(0)\n\ts_barrier" ::: "memory"); }
template <int CTRL> __device__ __forceinline__ float dppf(float v) {
    return __int_as_float(__builtin_amdgcn_update_dpp(0, __float_as_int(v), CTRL, 0xf, 0xf, true));
}
template <int CTRL> __device__ __forceinline__ unsigned dppu(unsigned v) {
    return (unsigned)__builtin_amdgcn_update_dpp(0, (int)v, CTRL, 0xf, 0xf, true);
}
__device__ __forceinline__ float row_sum16(float v) {
    v += dppf<0x128>(v); v += dppf<0x124>(v); v += dppf<0x122>(v); v += dppf<0x121>(v); return v;
}
__device__ __forceinline__ float wave_sum(float v) {
    v = row_sum16(v);
    auto a = __builtin_amdgcn_permlane16_swap(__float_as_uint(v), __float_as_uint(v), false, false);
    v = __uint_as_float(a[0]) + __uint_as_float(a[1]);
    auto b = __builtin_amdgcn_permlane32_swap(__float_as_uint(v), __float_as_uint(v), false, false);
    return __uint_as_float(b[0]) + __uint_as_float(b[1]);
}
__device__ __forceinline__ unsigned wave_maxu(unsigned v) {
    v = max(v, dppu<0x128>(v)); v = max(v, dppu<0x124>(v)); v = max(v, dppu<0x122>(v)); v = max(v, dppu<0x121>(v));
    auto a = __builtin_amdgcn_permlane16_swap(v, v, false, false); v = max(a[0], a[1]);
    auto b = __builtin_amdgcn_permlane32_swap(v, v, false, false); return max(b[0], b[1]);
}
__device__ __forceinline__ u32x4 pack_pair(f32x4 a, f32x4 b) {
    auto x = __builtin_amdgcn_permlane16_swap(cvtpk(a[0], a[1]), cvtpk(b[0], b[1]), false, false);
    auto y = __builtin_amdgcn_permlane16_swap(cvtpk(a[2], a[3]), cvtpk(b[2], b[3]), false, false);
    return (u32x4){x[0], y[0], x[1], y[1]};
}

#define STR2(x) #x
#define STR(x) STR2(x)
#define EX(r) "v_exp_f32 v" STR(r) ", v" STR(r) "\n\t"
#define QKM(S, K, Q) "v_mfma_f32_32x32x16_bf16 " S ", " K ", " Q ", 0\n\t"
#define PVM(O, V, P) "v_smfmac_f32_32x32x32_bf16 " O ", " V ", " P ", %[idx]\n\t"
#define UNIT_A(O, V0, V1, MID) EX(194) EX(195) EX(196) MID EX(197) EX(198) EX(199) EX(200) EX(201) PVM(O, V0, "v[192:199]") \
    EX(202) EX(203) EX(204) EX(205) EX(206) EX(207) EX(208) EX(209) PVM(O, V1, "v[200:207]")
#define UNIT_B(O, V0, V1, MID) EX(210) EX(211) EX(212) MID EX(213) EX(214) EX(215) EX(216) EX(217) PVM(O, V0, "v[208:215]") \
    EX(218) EX(219) EX(220) EX(221) EX(222) EX(223) EX(192) EX(193) PVM(O, V1, "v[216:223]")
#define UNIT_B_LAST(O, V0, V1) EX(210) EX(211) EX(212) EX(213) EX(214) EX(215) EX(216) EX(217) PVM(O, V0, "v[208:215]") \
    EX(218) EX(219) EX(220) EX(221) EX(222) EX(223) "s_nop 1\n\t" PVM(O, V1, "v[216:223]")
#define SA "v[192:207]"
#define SB "v[208:223]"
#define S_CLOB "v192", "v193", "v194", "v195", "v196", "v197", "v198", "v199", "v200", "v201", "v202", "v203", "v204", "v205", "v206", "v207", \
    "v208", "v209", "v210", "v211", "v212", "v213", "v214", "v215", "v216", "v217", "v218", "v219", "v220", "v221", "v222", "v223"
#define LOADT(K, V0, V1) "s_add_u32 %[so], %[so], 0x2000\n\ts_and_b32 %[so], %[so], 0x1ffff\n\t" \
    "buffer_load_dwordx4 " K ", %[ko], %[rs], %[so] offen sc1\n\t" \
    "s_mov_b64 exec, %[vm]\n\t" \
    "buffer_load_dwordx4 " V0 ", %[vo], %[rs], %[so] offen offset:1024 sc1\n\t" \
    "buffer_load_dwordx4 " V1 ", %[vo], %[rs], %[so] offen offset:1280 sc1\n\t" \
    "s_mov_b64 exec, -1\n\t"
#define TILE(K, V0, V1, KN, KL, VL0, VL1) \
    UNIT_A("%[o0]", V0, V1, QKM(SB, K, "%[q1]") LOADT(KL, VL0, VL1)) \
    UNIT_B("%[o1]", V0, V1, QKM(SA, K, "%[q2]")) \
    UNIT_A("%[o2]", V0, V1, QKM(SB, K, "%[q3]")) \
    UNIT_B("%[o3]", V0, V1, "s_waitcnt vmcnt(3)\n\t" QKM(SA, KN, "%[q0]"))

template <int MODE>
__global__ __launch_bounds__(512, 2) void mix_kernel(Params p) {
    __shared__ __attribute__((aligned(16))) char smem_[LDS_BYTES];
    LAS char* smem = (LAS char*)smem_;
    const unsigned lds0 = (unsigned)(uintptr_t)smem;
    const int tid = threadIdx.x, lane = tid & 63;
    const int w = __builtin_amdgcn_readfirstlane(tid >> 6);
    const int xcd = blockIdx.x & 7, jj = blockIdx.x >> 3;
    const int tb = 2 * xcd + (jj & 1), hp = jj >> 1;
    const int kq = w & 3, hh = w >> 2, head = 2 * hp + hh;
    const int g4 = lane >> 4, t16 = lane & 15;
    char* const ws = p.ws;
    const __amdgpu_buffer_rsrc_t rs_kv = __builtin_amdgcn_make_buffer_rsrc(ws + WS_KV + (size_t)head * 131072 + kq * 2048, 0, 131072 - kq * 2048, 0x00020000);

    u32x4 kf, vc;
    u64 tag = 0;

    if constexpr (MODE == 0 || MODE == 1) {
        const int tokr = 128 * tb + 8 * hp + w;
        u32x4 rx, rw, rgw;
        asm volatile("global_load_dwordx4 %0, %3, %4\n\tglobal_load_dwordx4 %1, %3, %5\n\tglobal_load_dwordx4 %2, %3, %6"
                     : "=&v"(rx), "=&v"(rw), "=&v"(rgw) : "v"(lane * 16), "s"(p.x + (size_t)tokr * 512), "s"(p.wr), "s"(p.gw) : "memory");
        unsigned xo0, xo1, wo0, wo1;
        { const int t0 = 16 * w + (lane >> 3), t1 = t0 + 8;
          xo0 = t0 * 1024 + (((lane & 7) ^ ((t0 >> 1) & 7)) << 4);
          xo1 = t1 * 1024 + (((lane & 7) ^ ((t1 >> 1) & 7)) << 4);
          const int k0 = 16 * (w & 3) + (lane >> 2), pc = ((lane & 3) ^ (((k0 >> 3) & 1) << 1)) << 4;
          wo0 = k0 * 3072 + (w >> 2) * 1024 + pc;
          const int k1 = 8 * w + ((lane >> 2) & 7);
          wo1 = k1 * 3072 + 2048 + (((lane & 3) ^ ((w & 1) << 1)) << 4); }
        const char* xb = (const char*)p.x + (size_t)tb * 131072;
        const char* wb = (const char*)p.wqkv + hp * 64;
        auto issue = [&](int c) {
            const unsigned s = lds0 + (c % NSLOT) * SLOT;
            dma16(xb + c * 128, xo0, s + w * 2048); dma16(xb + c * 128, xo1, s + w * 2048 + 1024);
            dma16(wb + c * 196608, wo0, s + 16384 + w * 1024);
            dma16_half(wb + c * 196608, wo1, s + 16384 + 8192 + w * 512);
        };
#pragma unroll
        for (int c = 0; c < NSLOT; ++c) issue(c);
        asm volatile("s_waitcnt vmcnt(20)" : "+v"(rx), "+v"(rw), "+v"(rgw) :: "memory");
        { float ra = 0.f, ga = 0.f;
#pragma unroll
          for (int j = 0; j < 4; ++j) {
              const float a = bflo(rx[j]), b = bfhi(rx[j]);
              ra = fmaf(a, bflo(rw[j]), ra); ra = fmaf(b, bfhi(rw[j]), ra);
              const unsigned pr = cvtpk(a * bflo(rgw[j]), b * bfhi(rgw[j]));
              ga += bflo(pr); ga += bfhi(pr); }
          ra = wave_sum(ra); ga = wave_sum(ga);
          const unsigned rb = cvtpk(ra, 0.f) & 0xffffu;
          const unsigned key = (((rb & 0x8000u) ? (~rb & 0xffffu) : (rb | 0x8000u)) << 11) | (unsigned)(2047 - tokr);
          const float gv = bfr(sigmoidf_(bfr(ga)));
          if (lane == 0) { *(LAS unsigned*)(smem + L_RK + 4 * w) = key;
              __hip_atomic_store((GAS float*)(ws + WS_G) + tokr, gv, RLX_AGENT); } }

        f32x4 aq[2] = {}, ak[2] = {}, av[2] = {};
        const int fsw = (t16 >> 1) & 7;
        const int xa = (32 * kq + t16) * 128 + ((g4 ^ fsw) << 4);
        const int wa = 16384 + g4 * 512 + ((lane & 15) >> 2) * 64 + ((hh ^ (g4 & 1)) << 5) + (lane & 3) * 8;
#pragma unroll
        for (int c = 0; c < 8; ++c) {
            constexpr int YOUNG[8] = {4, 3, 3, 3, 3, 2, 1, 0};
            switch (YOUNG[c]) { case 4: wait_bar<16>(); break; case 3: wait_bar<12>(); break; case 2: wait_bar<8>(); break; case 1: wait_bar<4>(); break; default: wait_bar<0>(); }
            if (c >= 1 && c + 4 < 8) issue(c + 4);
            if (c == 0 && w == 0) {
                const unsigned kk = wave_maxu(*(const LAS unsigned*)(smem + L_RK + 4 * (lane & 7)));
                if (lane == 0) __hip_atomic_store((GAS u64*)(ws + WS_RG) + blockIdx.x, ((u64)kk << 37) | (tag & ((1ull << 37) - 1)), RLX_AGENT);
            }
            const LAS char* s = smem + (c % NSLOT) * SLOT;
#pragma unroll
            for (int kk = 0; kk < 2; ++kk) {
                const u32x4 x0 = *(const LAS u32x4*)(s + (xa ^ (kk * 64))), x1 = *(const LAS u32x4*)(s + (xa ^ (kk * 64)) + 2048);
                const u32x4 fq = trd2(s + wa + kk * 2048, 256), fk = trd2(s + wa + 4096 + kk * 2048, 256), fv = trd2(s + wa + 8192 + kk * 2048, 256);
                aq[0] = mfma16(fq, x0, aq[0]); aq[1] = mfma16(fq, x1, aq[1]);
                ak[0] = mfma16(fk, x0, ak[0]); ak[1] = mfma16(fk, x1, ak[1]);
                av[0] = mfma16(x0, fv, av[0]); av[1] = mfma16(x1, fv, av[1]);
            }
        }
        constexpr float QS = 0.25f * 1.4426950408889634f;
        const u32x4 qf = pack_pair(aq[0] * QS, aq[1] * QS);
        kf = pack_pair(ak[0], ak[1]); vc = pack_pair(av[0], av[1]);
        __builtin_amdgcn_raw_buffer_store_b128(kf, rs_kv, lane * 16, tb * 8192, 16);
        __builtin_amdgcn_raw_buffer_store_b128(vc, rs_kv, lane * 16 + 1024, tb * 8192, 16);
        if constexpr (MODE == 1) *(u32x4*)(ws + WS_Q + (size_t)head * 65536 + (4 * tb + kq) * 1024 + lane * 16) = qf;
        else *(LAS u32x4*)(smem + L_Q + w * 1024 + lane * 16) = qf;
    }

    int sel = 0; float rsel = 0.f;
    if constexpr (MODE == 0 || MODE == 2) {
        u32x4 q0, q1, q2, q3;
        if constexpr (MODE == 2) {
            const char* qb = ws + WS_Q + (size_t)head * 65536 + tb * 4096 + lane * 16;
            q0 = *(const u32x4*)(qb + kq * 1024); q1 = *(const u32x4*)(qb + ((kq + 1) & 3) * 1024);
            q2 = *(const u32x4*)(qb + ((kq + 2) & 3) * 1024); q3 = *(const u32x4*)(qb + ((kq + 3) & 3) * 1024);
            kf = __builtin_amdgcn_raw_buffer_load_b128(rs_kv, lane * 16, tb * 8192, 0);
            vc = __builtin_amdgcn_raw_buffer_load_b128(rs_kv, lane * 16 + 1024, tb * 8192, 0);
        }
        const bool ffn = jj < 28; const int fi = 28 * xcd + jj;
        u32x4 rg0 = *(const u32x4*)(ws + WS_RG + lane * 32), rg1 = *(const u32x4*)(ws + WS_RG + lane * 32 + 16);
        u32x4 wgv = {}, wuv = {}; unsigned wdv[8] = {};
        if (ffn) {
            wgv = *(const u32x4*)(p.wg + (size_t)tid * 1792 + 8 * fi); wuv = *(const u32x4*)(p.wu + (size_t)tid * 1792 + 8 * fi);
#pragma unroll
            for (int f = 0; f < 8; ++f) wdv[f] = p.wd[(size_t)(8 * fi + f) * 512 + tid];
        }
        const unsigned pat = ((lane & 11) == 0) ? 0x3f803f80u : 0u;
        u32x4 xv0, xv1;
#pragma unroll
        for (int j = 0; j < 4; ++j) { auto r = __builtin_amdgcn_permlane16_swap(vc[j], pat, false, false); xv0[j] = r[0]; xv1[j] = r[1]; }
        f32x16 o0 = {}, o1 = {}, o2 = {}, o3 = {};
        const int idx = 0xdddd;
        asm volatile(
            "s_nop 1\n\t" QKM(SA, "%[k]", "%[q0]") "s_nop 11\n\t" EX(192) EX(193)
            UNIT_A("%[o0]", "%[v0]", "%[v1]", QKM(SB, "%[k]", "%[q1]"))
            UNIT_B("%[o1]", "%[v0]", "%[v1]", QKM(SA, "%[k]", "%[q2]"))
            UNIT_A("%[o2]", "%[v0]", "%[v1]", QKM(SB, "%[k]", "%[q3]"))
            UNIT_B_LAST("%[o3]", "%[v0]", "%[v1]") "s_nop 11"
            : [o0] "+v"(o0), [o1] "+v"(o1), [o2] "+v"(o2), [o3] "+v"(o3)
            : [k] "v"(kf), [v0] "v"(xv0), [v1] "v"(xv1), [q0] "v"(q0), [q1] "v"(q1), [q2] "v"(q2), [q3] "v"(q3), [idx] "v"(idx)
            : S_CLOB);
        { const unsigned k0 = max(max(rg0[1], rg0[3]), max(rg1[1], rg1[3])) >> 5;
          const unsigned kk = __builtin_amdgcn_readfirstlane(wave_maxu(k0));
          sel = 2047 - (int)(kk & 2047u);
          const unsigned mono = kk >> 11, rb = (mono & 0x8000u) ? (mono & 0x7fffu) : (~mono & 0xffffu);
          rsel = bfr(sigmoidf_(bflo(rb))); }
        const unsigned xs = ffn ? (unsigned)p.x[(size_t)sel * 512 + tid] : 0u;
        const int vo = (lane >> 5) * 512 + (lane & 15) * 16;
        u32x4 yk, yv0, yv1, zk, zv0, zv1;
        yv0 = yv1 = zv0 = zv1 = (u32x4){pat, pat, pat, pat};
        { const int s1 = ((tb + 1) & 15) * 8192, s2 = ((tb + 2) & 15) * 8192;
          yk = __builtin_amdgcn_raw_buffer_load_b128(rs_kv, lane * 16, s1, 16);
          zk = __builtin_amdgcn_raw_buffer_load_b128(rs_kv, lane * 16, s2, 16);
          if ((lane & 16) == 0) {
              yv0 = __builtin_amdgcn_raw_buffer_load_b128(rs_kv, vo + 1024, s1, 16); yv1 = __builtin_amdgcn_raw_buffer_load_b128(rs_kv, vo + 1280, s1, 16);
              zv0 = __builtin_amdgcn_raw_buffer_load_b128(rs_kv, vo + 1024, s2, 16); zv1 = __builtin_amdgcn_raw_buffer_load_b128(rs_kv, vo + 1280, s2, 16); } }
        if (ffn) {
            const float xf = bflo(xs);
            float a[16];
#pragma unroll
            for (int j = 0; j < 4; ++j) { a[2 * j] = xf * bflo(wgv[j]); a[2 * j + 1] = xf * bfhi(wgv[j]); a[8 + 2 * j] = xf * bflo(wuv[j]); a[9 + 2 * j] = xf * bfhi(wuv[j]); }
#pragma unroll
            for (int j = 0; j < 8; ++j) { auto r = __builtin_amdgcn_permlane32_swap(__float_as_uint(a[j]), __float_as_uint(a[j + 8]), false, false); a[j] = __uint_as_float(r[0]) + __uint_as_float(r[1]); }
#pragma unroll
            for (int j = 0; j < 4; ++j) { auto r = __builtin_amdgcn_permlane16_swap(__float_as_uint(a[j]), __float_as_uint(a[j + 4]), false, false); a[j] = __uint_as_float(r[0]) + __uint_as_float(r[1]); }
#pragma unroll
            for (int j = 0; j < 4; ++j) a[j] = row_sum16(a[j]);
            if (t16 < 4) *(LAS float*)(smem + L_FS + ((4 * g4 + t16) * 8 + w) * 4) = t16 == 0 ? a[0] : t16 == 1 ? a[1] : t16 == 2 ? a[2] : a[3];
            lds_bar();
            { const f32x4 g0 = *(const LAS f32x4*)(smem + L_FS + w * 32), g1 = *(const LAS f32x4*)(smem + L_FS + w * 32 + 16);
              const f32x4 u0 = *(const LAS f32x4*)(smem + L_FS + 256 + w * 32), u1 = *(const LAS f32x4*)(smem + L_FS + 256 + w * 32 + 16);
              const float gs = bfr(((g0[0] + g0[1]) + (g0[2] + g0[3])) + ((g1[0] + g1[1]) + (g1[2] + g1[3])));
              const float us = bfr(((u0[0] + u0[1]) + (u0[2] + u0[3])) + ((u1[0] + u1[1]) + (u1[2] + u1[3])));
              const float hv = bfr(bfr(gs * sigmoidf_(gs)) * us);
              if (lane == 0) *(LAS float*)(smem + L_FH + 4 * w) = hv; }
            lds_bar();
            { const f32x4 h0 = *(const LAS f32x4*)(smem + L_FH), h1 = *(const LAS f32x4*)(smem + L_FH + 16);
              float y = 0.f;
#pragma unroll
              for (int f = 0; f < 4; ++f) { y = fmaf(h0[f], bflo(wdv[f]), y); y = fmaf(h1[f], bflo(wdv[f + 4]), y); }
              __hip_atomic_store((GAS float*)(ws + WS_YP) + fi * 512 + tid, y, RLX_AGENT); }
        }
        { int so = ((tb + 2) & 15) * 8192, cnt = 5;
          const u64 vm = 0x0000ffff0000ffffull;
          asm volatile(
              "s_nop 1\n\t" QKM(SA, "%[yk]", "%[q0]") "s_nop 11\n\t" EX(192) EX(193)
              "1:\n\t"
              TILE("%[yk]", "%[yv0]", "%[yv1]", "%[zk]", "%[xk]", "%[xv0]", "%[xv1]")
              TILE("%[zk]", "%[zv0]", "%[zv1]", "%[xk]", "%[yk]", "%[yv0]", "%[yv1]")
              TILE("%[xk]", "%[xv0]", "%[xv1]", "%[yk]", "%[zk]", "%[zv0]", "%[zv1]")
              "s_sub_u32 %[cnt], %[cnt], 1\n\ts_cmp_lg_u32 %[cnt], 0\n\ts_cbranch_scc1 1b\n\t"
              "s_waitcnt vmcnt(0)\n\ts_nop 11"
              : [o0] "+v"(o0), [o1] "+v"(o1), [o2] "+v"(o2), [o3] "+v"(o3),
                [xk] "+v"(kf), [xv0] "+v"(xv0), [xv1] "+v"(xv1), [yk] "+v"(yk), [yv0] "+v"(yv0), [yv1] "+v"(yv1),
                [zk] "+v"(zk), [zv0] "+v"(zv0), [zv1] "+v"(zv1), [so] "+s"(so), [cnt] "+s"(cnt)
              : [q0] "v"(q0), [q1] "v"(q1), [q2] "v"(q2), [q3] "v"(q3), [idx] "v"(idx), [ko] "v"(lane * 16), [vo] "v"(vo),
                [rs] "s"(rs_kv), [vm] "s"(vm)
              : S_CLOB, "scc", "memory"); }
        { auto put = [&](const f32x16& o, int j) {
              LAS char* d = smem + L_CB + ((hh * 4 + ((kq + j) & 3)) * 3 + (j - 1)) * 2304;
              *(LAS f32x4*)(d + lane * 16) = (f32x4){o[0], o[1], o[2], o[3]};
              *(LAS f32x4*)(d + 1024 + lane * 16) = (f32x4){o[4], o[5], o[6], o[7]};
              *(LAS float*)(d + 2048 + lane * 4) = o[8]; };
          put(o1, 1); put(o2, 2); put(o3, 3); }
        lds_bar();
        float o[9];
#pragma unroll
        for (int r = 0; r < 9; ++r) o[r] = o0[r];
#pragma unroll
        for (int j = 0; j < 3; ++j) {
            const LAS char* d = smem + L_CB + (w * 3 + j) * 2304;
            const f32x4 a = *(const LAS f32x4*)(d + lane * 16), b = *(const LAS f32x4*)(d + 1024 + lane * 16);
#pragma unroll
            for (int r = 0; r < 4; ++r) { o[r] += a[r]; o[4 + r] += b[r]; }
            o[8] += *(const LAS float*)(d + 2048 + lane * 4);
        }
        { float mag = 0.f;
#pragma unroll
          for (int r = 0; r < 8; ++r) mag += fabsf(o[r]);
          const bool safe = o[8] >= 9.313225746154785e-10f && o[8] <= 1.8446744073709552e19f && mag <= 3.0e38f;
          if (!__all(safe)) {
              f32x16 acc = {}; float m = -INFINITY;
              u32x4 v0 = {pat, pat, pat, pat}, v1 = v0;
              const __amdgpu_buffer_rsrc_t rs_all = __builtin_amdgcn_make_buffer_rsrc(ws + WS_KV + (size_t)head * 131072, 0, 131072, 0x00020000);
              for (int t = 0; t < 64; ++t) {
                  const u32x4 kk = __builtin_amdgcn_raw_buffer_load_b128(rs_all, lane * 16, t * 2048, 16);
                  if ((lane & 16) == 0) { v0 = __builtin_amdgcn_raw_buffer_load_b128(rs_all, vo + 1024, t * 2048, 16); v1 = __builtin_amdgcn_raw_buffer_load_b128(rs_all, vo + 1280, t * 2048, 16); }
                  f32x16 s = __builtin_amdgcn_mfma_f32_32x32x16_bf16(__builtin_bit_cast(bf16x8, kk), __builtin_bit_cast(bf16x8, q0), (f32x16){}, 0, 0, 0);
                  float tm = s[0];
#pragma unroll
                  for (int r = 1; r < 16; ++r) tm = fmaxf(tm, s[r]);
                  auto sw = __builtin_amdgcn_permlane32_swap(__float_as_uint(tm), __float_as_uint(tm), false, false);
                  const float mn = fmaxf(m, fmaxf(__uint_as_float(sw[0]), __uint_as_float(sw[1])));
                  const float al = __builtin_amdgcn_exp2f(m - mn); m = mn;
#pragma unroll
                  for (int r = 0; r < 16; ++r) { s[r] = __builtin_amdgcn_exp2f(s[r] - mn); acc[r] *= al; }
                  acc = __builtin_amdgcn_smfmac_f32_32x32x32_bf16(__builtin_bit_cast(bf16x8, v0), __builtin_bit_cast(bf16x16, __builtin_shufflevector(s, s, 0, 1, 2, 3, 4, 5, 6, 7)), acc, idx, 0, 0);
                  acc = __builtin_amdgcn_smfmac_f32_32x32x32_bf16(__builtin_bit_cast(bf16x8, v1), __builtin_bit_cast(bf16x16, __builtin_shufflevector(s, s, 8, 9, 10, 11, 12, 13, 14, 15)), acc, idx, 0, 0);
              }
#pragma unroll
              for (int r = 0; r < 9; ++r) o[r] = acc[r];
          } }
        { const float inv = 1.0f / o[8];
          const u32x4 of = {cvtpk(o[0] * inv, o[1] * inv), cvtpk(o[2] * inv, o[3] * inv), cvtpk(o[4] * inv, o[5] * inv), cvtpk(o[6] * inv, o[7] * inv)};
          const __amdgpu_buffer_rsrc_t rs_o = __builtin_amdgcn_make_buffer_rsrc(ws + WS_O, 0, 2u << 20, 0x00020000);
          __builtin_amdgcn_raw_buffer_store_b128(of, rs_o, lane * 16, (4 * tb + kq) * 32768 + head * 1024, 16); }
    }

    if constexpr (MODE == 0 || MODE == 3) {
        if constexpr (MODE == 3) {
            const char* wob = (const char*)p.wo + hp * 64;
#pragma unroll
            for (int i = 0; i < 4; ++i) {
                const int k = 64 * w + 16 * i + (lane >> 2);
                dma16(wob, k * 1024 + (((lane & 3) ^ (((k >> 2) & 1) << 1)) << 4), lds0 + L_WO + (4 * w + i) * 1024);
            }
            const u32x4 rg0 = *(const u32x4*)(ws + WS_RG + lane * 32), rg1 = *(const u32x4*)(ws + WS_RG + lane * 32 + 16);
            const unsigned k0 = max(max(rg0[1], rg0[3]), max(rg1[1], rg1[3])) >> 5;
            const unsigned kk = __builtin_amdgcn_readfirstlane(wave_maxu(k0));
            sel = 2047 - (int)(kk & 2047u);
            const unsigned mono = kk >> 11, rb = (mono & 0x8000u) ? (mono & 0x7fffu) : (~mono & 0xffffu);
            rsel = bfr(sigmoidf_(bflo(rb)));
        }
        const int tok = 128 * tb + 16 * w + t16;
        const __amdgpu_buffer_rsrc_t rs_o = __builtin_amdgcn_make_buffer_rsrc(ws + WS_O + (size_t)(4 * tb + (w >> 1)) * 32768, 0, 32768, 0x00020000);
        u32x4 ob[16];
        { const int oo = (g4 >> 1) * 1024 + ((g4 & 1) * 32 + 16 * (w & 1) + t16) * 16;
#pragma unroll
          for (int kk = 0; kk < 16; ++kk) ob[kk] = __builtin_amdgcn_raw_buffer_load_b128(rs_o, oo, kk * 2048, 16); }
        const u32x2 xa = *(const u32x2*)(p.x + (size_t)tok * 512 + 32 * hp + 4 * g4), xb = *(const u32x2*)(p.x + (size_t)tok * 512 + 32 * hp + 16 + 4 * g4);
        const float gt = __hip_atomic_load((GAS float*)(ws + WS_G) + tok, RLX_AGENT);
        const bool own = (sel >> 7) == tb;
        if (own) {
            const __amdgpu_buffer_rsrc_t rs_y = __builtin_amdgcn_make_buffer_rsrc(ws + WS_YP + (size_t)w * 28 * 2048, 0, 28 * 2048, 0x00020000);
            f32x4 s = {};
#pragma unroll
            for (int i = 0; i < 4; ++i) s += __builtin_bit_cast(f32x4, __builtin_amdgcn_raw_buffer_load_b128(rs_y, (lane >> 3) * 2048 + hp * 128 + (lane & 7) * 16, i * 16384, 16));
#pragma unroll
            for (int r = 0; r < 4; ++r) {
                float v = s[r]; v += dppf<0x128>(v);
                auto a = __builtin_amdgcn_permlane16_swap(__float_as_uint(v), __float_as_uint(v), false, false); v = __uint_as_float(a[0]) + __uint_as_float(a[1]);
                auto b = __builtin_amdgcn_permlane32_swap(__float_as_uint(v), __float_as_uint(v), false, false); s[r] = __uint_as_float(b[0]) + __uint_as_float(b[1]); }
            if (lane < 8) *(LAS f32x4*)(smem + L_YS + w * 128 + lane * 16) = s;
        }
        wait_bar<0>();
        f32x4 c0 = {}, c1 = {};
        { const LAS char* wa = smem + L_WO + ((g4 >> 1) * 16 + (g4 & 1) * 4 + ((lane & 15) >> 2)) * 64 + (lane & 3) * 8;
          const int h0 = (g4 & 1) << 5, h1 = h0 ^ 32;
#pragma unroll
          for (int kk = 0; kk < 16; ++kk) {
              c0 = mfma16(trd2(wa + kk * 2048 + h0, 512), ob[kk], c0);
              c1 = mfma16(trd2(wa + kk * 2048 + h1, 512), ob[kk], c1); } }
        float bb[8] = {bflo(xa[0]), bfhi(xa[0]), bflo(xa[1]), bfhi(xa[1]), bflo(xb[0]), bfhi(xb[0]), bflo(xb[1]), bfhi(xb[1])};
        if (own && tok == sel) {
            f32x4 y0 = {}, y1 = {};
#pragma unroll
            for (int i = 0; i < 8; ++i) { y0 += *(const LAS f32x4*)(smem + L_YS + i * 128 + g4 * 16); y1 += *(const LAS f32x4*)(smem + L_YS + i * 128 + 64 + g4 * 16); }
#pragma unroll
            for (int r = 0; r < 4; ++r) { bb[r] = bfr(bb[r] + bfr(bfr(y0[r]) * rsel)); bb[4 + r] = bfr(bb[4 + r] + bfr(bfr(y1[r]) * rsel)); }
        }
        const float gm = bfr(1.0f - gt);
        f32x4 r0, r1;
#pragma unroll
        for (int r = 0; r < 4; ++r) { r0[r] = bfr(gt * bfr(c0[r])) + bfr(gm * bb[r]); r1[r] = bfr(gt * bfr(c1[r])) + bfr(gm * bb[4 + r]); }
        *(u32x4*)(p.out + (size_t)tok * 512 + 32 * hp + 16 * (g4 & 1) + 8 * (g4 >> 1)) = pack_pair(r0, r1);
    }
}

extern "C" void kernel_launch(void* const* d_in, const int* in_sizes, int n_in,
                              void* d_out, int out_size, void* d_ws, size_t ws_size,
                              hipStream_t stream) {
    Params p;
    p.x = (const uint16_t*)d_in[0]; p.wqkv = (const uint16_t*)d_in[1]; p.wo = (const uint16_t*)d_in[2];
    p.wr = (const uint16_t*)d_in[4]; p.wg = (const uint16_t*)d_in[5]; p.wu = (const uint16_t*)d_in[6];
    p.wd = (const uint16_t*)d_in[7]; p.gw = (const uint16_t*)d_in[8];
    p.out = (uint16_t*)d_out; p.ws = (char*)d_ws;
    mix_kernel<1><<<256, 512, 0, stream>>>(p);
    mix_kernel<2><<<256, 512, 0, stream>>>(p);
    mix_kernel<3><<<256, 512, 0, stream>>>(p);
}
```

```cpp
#include <hip/hip_runtime.h>
#include <hip/hip_bf16.h>
#include <stdint.h>

typedef __attribute__((ext_vector_type(8))) __bf16 bf16x8;
typedef __attribute__((ext_vector_type(16))) __bf16 bf16x16;
typedef __attribute__((ext_vector_type(4))) float f32x4;
typedef __attribute__((ext_vector_type(8))) float f32x8;
typedef __attribute__((ext_vector_type(16))) float f32x16;
typedef __attribute__((ext_vector_type(4))) unsigned u32x4;
typedef __attribute__((ext_vector_type(2))) unsigned u32x2;
typedef __attribute__((ext_vector_type(4))) short s16x4;
typedef unsigned long long u64;
#define LAS __attribute__((address_space(3)))
#define GAS __attribute__((address_space(1)))
#define RLX_AGENT __ATOMIC_RELAXED, __HIP_MEMORY_SCOPE_AGENT

constexpr size_t WS_KV = 0;
constexpr size_t WS_O = 4u << 20;
constexpr size_t WS_YP = 6u << 20;
constexpr size_t WS_G = WS_YP + (512u << 10);
constexpr size_t WS_RG = WS_G + 8192;
constexpr size_t WS_KVF = WS_RG + 2048;
constexpr size_t WS_OF = WS_KVF + 16384;
constexpr size_t WS_YF = WS_OF + 16384;
constexpr size_t WS_EP = WS_YF + 16384;

constexpr int SLOT = 28672, NSLOT = 5;
constexpr int L_MISC = SLOT * NSLOT;
constexpr int L_RK = L_MISC;
constexpr int L_FS = L_MISC + 64;
constexpr int L_FH = L_FS + 512;
constexpr int L_YS = L_FH + 64;
constexpr int L_Q = L_MISC + 2048;
constexpr int LDS_BYTES = L_Q + 8192;
constexpr int L_WO = 3 * SLOT;
constexpr int L_CB = 0;
constexpr int SPIN_CAP = 200000;

#define STAMP(i)
#define STAMP_DECL
#define STAMP_OUT

struct Params {
    const uint16_t* x; const uint16_t* wqkv; const uint16_t* wo; const uint16_t* wr;
    const uint16_t* wg; const uint16_t* wu; const uint16_t* wd; const uint16_t* gw;
    uint16_t* out; char* ws;
};

__device__ __forceinline__ float bflo(unsigned u) { return __uint_as_float(u << 16); }
__device__ __forceinline__ float bfhi(unsigned u) { return __uint_as_float(u & 0xffff0000u); }
__device__ __forceinline__ unsigned cvtpk(float lo, float hi) {
    typedef __attribute__((ext_vector_type(2))) float f2; typedef __attribute__((ext_vector_type(2))) __bf16 b2;
    return __builtin_bit_cast(unsigned, __builtin_convertvector((f2){lo, hi}, b2));
}
__device__ __forceinline__ float bfr(float v) { return bflo(cvtpk(v, 0.f) & 0xffffu); }
__device__ __forceinline__ float sigmoidf_(float v) { return __builtin_amdgcn_rcpf(1.0f + __builtin_amdgcn_exp2f(v * -1.4426950408889634f)); }
__device__ __forceinline__ f32x4 mfma16(u32x4 a, u32x4 b, f32x4 c) {
    return __builtin_amdgcn_mfma_f32_16x16x32_bf16(__builtin_bit_cast(bf16x8, a), __builtin_bit_cast(bf16x8, b), c, 0, 0, 0);
}
__device__ __forceinline__ u32x2 trd(const LAS char* p) {
    return __builtin_bit_cast(u32x2, __builtin_amdgcn_ds_read_tr16_b64_v4i16((LAS s16x4*)p));
}
__device__ __forceinline__ u32x4 trd2(const LAS char* p, int d) { u32x2 a = trd(p), b = trd(p + d); return (u32x4){a[0], a[1], b[0], b[1]}; }
__device__ __forceinline__ void dma16(const void* sbase, unsigned voff, unsigned lds) {
    unsigned keep;
    asm volatile("s_mov_b32 %0, m0\n\ts_mov_b32 m0, %3\n\ts_nop 0\n\tglobal_load_lds_dwordx4 %1, %2\n\ts_mov_b32 m0, %0"
                 : "=&s"(keep) : "v"(voff), "s"(sbase), "s"(lds) : "memory");
}
__device__ __forceinline__ void dma16_half(const void* sbase, unsigned voff, unsigned lds) {
    unsigned keep;
    asm volatile("s_mov_b32 %0, m0\n\ts_mov_b32 m0, %3\n\ts_mov_b32 exec_hi, 0\n\tglobal_load_lds_dwordx4 %1, %2\n\ts_mov_b32 exec_hi, -1\n\ts_mov_b32 m0, %0"
                 : "=&s"(keep) : "v"(voff), "s"(sbase), "s"(lds) : "memory");
}
template <int N> __device__ __forceinline__ void wait_bar() {
    asm volatile("" ::: "memory"); __builtin_amdgcn_s_waitcnt(0xc07f);
    asm volatile("s_waitcnt vmcnt(%0) lgkmcnt(0)\n\ts_barrier" :: "n"(N) : "memory"); }
__device__ __forceinline__ void lds_bar() { asm volatile("s_waitcnt lgkmcnt(0)\n\ts_barrier" ::: "memory"); }
__device__ __forceinline__ void bare_bar() { asm volatile("s_barrier" ::: "memory"); }
template <int CTRL> __device__ __forceinline__ float dppf(float v) {
    return __int_as_float(__builtin_amdgcn_update_dpp(0, __float_as_int(v), CTRL, 0xf, 0xf, true));
}
template <int CTRL> __device__ __forceinline__ unsigned dppu(unsigned v) {
    return (unsigned)__builtin_amdgcn_update_dpp(0, (int)v, CTRL, 0xf, 0xf, true);
}
__device__ __forceinline__ float row_sum16(float v) {
    v += dppf<0x128>(v); v += dppf<0x124>(v); v += dppf<0x122>(v); v += dppf<0x121>(v); return v;
}
__device__ __forceinline__ float wave_sum(float v) {
    v = row_sum16(v);
    auto a = __builtin_amdgcn_permlane16_swap(__float_as_uint(v), __float_as_uint(v), false, false);
    v = __uint_as_float(a[0]) + __uint_as_float(a[1]);
    auto b = __builtin_amdgcn_permlane32_swap(__float_as_uint(v), __float_as_uint(v), false, false);
    return __uint_as_float(b[0]) + __uint_as_float(b[1]);
}
__device__ __forceinline__ unsigned wave_maxu(unsigned v) {
    v = max(v, dppu<0x128>(v)); v = max(v, dppu<0x124>(v)); v = max(v, dppu<0x122>(v)); v = max(v, dppu<0x121>(v));
    auto a = __builtin_amdgcn_permlane16_swap(v, v, false, false); v = max(a[0], a[1]);
    auto b = __builtin_amdgcn_permlane32_swap(v, v, false, false); return max(b[0], b[1]);
}
__device__ __forceinline__ u32x4 pack_pair(f32x4 a, f32x4 b) {
    auto x = __builtin_amdgcn_permlane16_swap(cvtpk(a[0], a[1]), cvtpk(b[0], b[1]), false, false);
    auto y = __builtin_amdgcn_permlane16_swap(cvtpk(a[2], a[3]), cvtpk(b[2], b[3]), false, false);
    return (u32x4){x[0], y[0], x[1], y[1]};
}
__device__ __forceinline__ u64 poll_flags(const char* f, bool use, u64 need, u64 tag, int& spin) {
    for (;;) {
        const u64 v = __hip_atomic_load((GAS u64*)f, RLX_AGENT);
        const u64 m = __ballot(use && v == tag);
        if ((m & need) == need || spin <= 0) return m;
        --spin; __builtin_amdgcn_s_sleep(1);
    }
}

#define STR2(x) #x
#define STR(x) STR2(x)
#define EX(r) "v_exp_f32 v" STR(r) ", v" STR(r) "\n\t"
#define QKM(S, K, Q) "v_mfma_f32_32x32x16_bf16 " S ", " K ", " Q ", 0\n\t"
#define PVM(O, V, P) "v_smfmac_f32_32x32x32_bf16 " O ", " V ", " P ", %[idx]\n\t"
#define UNIT_A(O, V0, V1, MID) EX(194) EX(195) EX(196) MID EX(197) EX(198) EX(199) EX(200) EX(201) PVM(O, V0, "v[192:199]") \
    EX(202) EX(203) EX(204) EX(205) EX(206) EX(207) EX(208) EX(209) PVM(O, V1, "v[200:207]")
#define UNIT_B(O, V0, V1, MID) EX(210) EX(211) EX(212) MID EX(213) EX(214) EX(215) EX(216) EX(217) PVM(O, V0, "v[208:215]") \
    EX(218) EX(219) EX(220) EX(221) EX(222) EX(223) EX(192) EX(193) PVM(O, V1, "v[216:223]")
#define UNIT_B_LAST(O, V0, V1) EX(210) EX(211) EX(212) EX(213) EX(214) EX(215) EX(216) EX(217) PVM(O, V0, "v[208:215]") \
    EX(218) EX(219) EX(220) EX(221) EX(222) EX(223) "s_nop 1\n\t" PVM(O, V1, "v[216:223]")
#define SA "v[192:207]"
#define SB "v[208:223]"
#define S_CLOB "v192", "v193", "v194", "v195", "v196", "v197", "v198", "v199", "v200", "v201", "v202", "v203", "v204", "v205", "v206", "v207", \
    "v208", "v209", "v210", "v211", "v212", "v213", "v214", "v215", "v216", "v217", "v218", "v219", "v220", "v221", "v222", "v223"
#define LOADT(K, V0, V1) "s_add_u32 %[so], %[so], 0x2000\n\ts_and_b32 %[so], %[so], 0x1ffff\n\t" \
    "s_lshr_b32 %[t], %[so], 13\n\ts_bitcmp1_b32 %[rdy], %[t]\n\ts_cbranch_scc1 3f\n\t" \
    "2:\n\tglobal_load_dwordx2 v[224:225], %[fo], %[fb] sc1\n\ts_waitcnt vmcnt(0)\n\t" \
    "v_cmp_eq_u64 vcc, %[tag], v[224:225]\n\ts_nop 4\n\ts_and_b32 %[rdy], vcc_lo, 0xffff\n\t" \
    "s_bitcmp1_b32 %[rdy], %[t]\n\ts_cbranch_scc1 3f\n\t" \
    "s_sleep 1\n\ts_sub_i32 %[spin], %[spin], 1\n\ts_cmp_gt_i32 %[spin], 0\n\ts_cbranch_scc1 2b\n\t" \
    "3:\n\tbuffer_load_dwordx4 " K ", %[ko], %[rs], %[so] offen sc1\n\t" \
    "s_mov_b64 exec, %[vm]\n\t" \
    "buffer_load_dwordx4 " V0 ", %[ko], %[rs], %[so] offen offset:1024 sc1\n\t" \
    "buffer_load_dwordx4 " V1 ", %[ko], %[rs], %[so] offen offset:1280 sc1\n\t" \
    "s_mov_b64 exec, -1\n\t"
#define TILE(K, V0, V1, KN, KL, VL0, VL1, EXTRA) \
    UNIT_A("%[o0]", V0, V1, QKM(SB, K, "%[q1]") LOADT(KL, VL0, VL1)) \
    UNIT_B("%[o1]", V0, V1, QKM(SA, K, "%[q2]")) \
    UNIT_A("%[o2]", V0, V1, QKM(SB, K, "%[q3]")) \
    UNIT_B("%[o3]", V0, V1, "s_waitcnt vmcnt(3)\n\t" QKM(SA, KN, "%[q0]") EXTRA)
#define FLAG(FO, FB) "s_mov_b64 exec, 1\n\tglobal_store_dwordx2 " FO ", %[vtag], " FB " sc1\n\ts_mov_b64 exec, -1\n\t"

__global__ __launch_bounds__(512, 2) void mix_kernel(Params p) {
    __shared__ __attribute__((aligned(16))) char smem_[LDS_BYTES];
    LAS char* smem = (LAS char*)smem_;
    const unsigned lds0 = (unsigned)(uintptr_t)smem;
    const int tid = threadIdx.x, lane = tid & 63;
    const int w = __builtin_amdgcn_readfirstlane(tid >> 6);
    const int xcd = blockIdx.x & 7, jj = blockIdx.x >> 3;
    const int tb = 4 * (xcd >> 1) + (jj & 3), hp = 8 * (xcd & 1) + (jj >> 2);
    const int kq = w & 3, hh = w >> 2, head = 2 * hp + hh;
    const int g4 = lane >> 4, t16 = lane & 15;
    char* const ws = p.ws;
    const __amdgpu_buffer_rsrc_t rs_kv = __builtin_amdgcn_make_buffer_rsrc(ws + WS_KV + (size_t)head * 131072 + kq * 2048, 0, 131072 - kq * 2048, 0x00020000);
    int spin = SPIN_CAP;
    STAMP_DECL; STAMP(0);

    const int tokr = 128 * tb + 8 * hp + w;
    u32x4 rx, rw, rgw; u32x2 ep;
    asm volatile("global_load_dwordx4 %0, %4, %5\n\tglobal_load_dwordx4 %1, %4, %6\n\tglobal_load_dwordx4 %2, %4, %7\n\tglobal_load_dwordx2 %3, %8, %9 sc1"
                 : "=&v"(rx), "=&v"(rw), "=&v"(rgw), "=&v"(ep)
                 : "v"(lane * 16), "s"(p.x + (size_t)tokr * 512), "s"(p.wr), "s"(p.gw), "v"(0), "s"(ws + WS_EP) : "memory");
    unsigned xo0, xo1, wo0, wo1;
    { const int t0 = 16 * w + (lane >> 3), t1 = t0 + 8;
      xo0 = t0 * 1024 + (((lane & 7) ^ ((t0 >> 1) & 7)) << 4);
      xo1 = t1 * 1024 + (((lane & 7) ^ ((t1 >> 1) & 7)) << 4);
      const int k0 = 16 * (w & 3) + (lane >> 2), pc = ((lane & 3) ^ (((k0 >> 3) & 1) << 1)) << 4;
      wo0 = k0 * 3072 + (w >> 2) * 1024 + pc;
      const int k1 = 8 * w + ((lane >> 2) & 7);
      wo1 = k1 * 3072 + 2048 + (((lane & 3) ^ ((w & 1) << 1)) << 4); }
    const char* xb = (const char*)p.x + (size_t)tb * 131072;
    const char* wb = (const char*)p.wqkv + hp * 64;
    const int rot = (2 * (jj & 3) + (jj >> 2)) & 7;
    auto issue = [&](int c) {
        const unsigned s = lds0 + (c % NSLOT) * SLOT; const int cc = (c + rot) & 7;
        dma16(xb + cc * 128, xo0, s + w * 2048); dma16(xb + cc * 128, xo1, s + w * 2048 + 1024);
        dma16(wb + cc * 196608, wo0, s + 16384 + w * 1024);
        dma16_half(wb + cc * 196608, wo1, s + 16384 + 8192 + w * 512);
    };
#pragma unroll
    for (int c = 0; c < NSLOT; ++c) { issue(c); if (c + 1 < NSLOT) bare_bar(); }
    const int fi = min(28 * xcd + jj, 28 * xcd + 27); const bool ffn = jj < 28;
    unsigned pf;
    STAMP(1);
    asm volatile("s_waitcnt vmcnt(20)" : "+v"(rx), "+v"(rw), "+v"(rgw), "+v"(ep) :: "memory");
    const u64 tag = (((u64)(unsigned)__builtin_amdgcn_readfirstlane(ep[1]) << 32) | (unsigned)__builtin_amdgcn_readfirstlane(ep[0])) + 1;
    { float ra = 0.f, ga = 0.f;
#pragma unroll
      for (int j = 0; j < 4; ++j) {
          const float a = bflo(rx[j]), b = bfhi(rx[j]);
          ra = fmaf(a, bflo(rw[j]), ra); ra = fmaf(b, bfhi(rw[j]), ra);
          const unsigned pr = cvtpk(a * bflo(rgw[j]), b * bfhi(rgw[j]));
          ga += bflo(pr); ga += bfhi(pr); }
      ra = wave_sum(ra); ga = wave_sum(ga);
      const unsigned rb = cvtpk(ra, 0.f) & 0xffffu;
      const unsigned key = (((rb & 0x8000u) ? (~rb & 0xffffu) : (rb | 0x8000u)) << 11) | (unsigned)(2047 - tokr);
      const float gv = bfr(sigmoidf_(bfr(ga)));
      if (lane == 0) { *(LAS unsigned*)(smem + L_RK + 4 * w) = key;
          __hip_atomic_store((GAS float*)(ws + WS_G) + tokr, gv, RLX_AGENT); } }
    STAMP(2);

    u32x4 kf, vc, qf;
    {   f32x4 aq[2] = {}, ak[2] = {}, av[2] = {};
        const int fsw = (t16 >> 1) & 7;
        const int xa = (32 * kq + t16) * 128 + ((g4 ^ fsw) << 4);
        const int wa = 16384 + g4 * 512 + ((lane & 15) >> 2) * 64 + ((hh ^ (g4 & 1)) << 5) + (lane & 3) * 8;
        u32x4 fx[2][4], fw[2][6];
        auto frags = [&](int c) {
            const LAS char* s = smem + (c % NSLOT) * SLOT;
#pragma unroll
            for (int kk = 0; kk < 2; ++kk) {
                fx[c & 1][2 * kk] = *(const LAS u32x4*)(s + (xa ^ (kk * 64))); fx[c & 1][2 * kk + 1] = *(const LAS u32x4*)(s + (xa ^ (kk * 64)) + 2048);
#pragma unroll
                for (int m = 0; m < 3; ++m) fw[c & 1][3 * kk + m] = trd2(s + wa + m * 4096 + kk * 2048, 256);
            } };
        wait_bar<16>();
        STAMP(3);
        if (w == 0) {
            const unsigned kk = wave_maxu(*(const LAS unsigned*)(smem + L_RK + 4 * (lane & 7)));
            if (lane == 0) __hip_atomic_store((GAS u64*)(ws + WS_RG) + blockIdx.x, ((u64)kk << 37) | (tag & ((1ull << 37) - 1)), RLX_AGENT);
        }
        frags(0);
#pragma unroll
        for (int c = 0; c < 8; ++c) {
            if (c < 4) wait_bar<12>(); else if (c == 4) wait_bar<9>(); else if (c == 5) wait_bar<5>(); else if (c == 6) wait_bar<1>();
            if (c == 3) { STAMP(7); }
            if (c < 3) issue(c + 5);
            if (c == 3) {
                const char* a;
                if (w == 0) a = (const char*)p.wo + (jj * 16 + (lane >> 2)) * 1024 + (xcd & 1) * 512 + (lane & 3) * 128;
                else if (w < 3) { const int u = tid - 64; a = (const char*)((u & 4) ? p.wu : p.wg) + (jj * 16 + (u >> 3)) * 3584 + xcd * 448 + (u & 3) * 128; }
                else { const int u = min(lane, 55); a = (const char*)p.wd + (224 * xcd + 7 * jj + (u >> 3)) * 1024 + (w == 3 ? (u & 7) * 128 : 0); }
                asm volatile("global_load_dword %0, %1, off" : "=v"(pf) : "v"(a) : "memory");
            }
            if (c < 7) frags(c + 1);
            __builtin_amdgcn_sched_barrier(0);
#pragma unroll
            for (int kk = 0; kk < 2; ++kk) {
                const u32x4 x0 = fx[c & 1][2 * kk], x1 = fx[c & 1][2 * kk + 1];
                aq[0] = mfma16(fw[c & 1][3 * kk], x0, aq[0]); aq[1] = mfma16(fw[c & 1][3 * kk], x1, aq[1]);
                ak[0] = mfma16(fw[c & 1][3 * kk + 1], x0, ak[0]); ak[1] = mfma16(fw[c & 1][3 * kk + 1], x1, ak[1]);
                av[0] = mfma16(x0, fw[c & 1][3 * kk + 2], av[0]); av[1] = mfma16(x1, fw[c & 1][3 * kk + 2], av[1]);
            }
            __builtin_amdgcn_sched_barrier(0);
        }
        constexpr float QS = 0.25f * 1.4426950408889634f;
        kf = pack_pair(ak[0], ak[1]); vc = pack_pair(av[0], av[1]);
        __builtin_amdgcn_raw_buffer_store_b128(kf, rs_kv, lane * 16, tb * 8192, 16);
        __builtin_amdgcn_raw_buffer_store_b128(vc, rs_kv, lane * 16 + 1024, tb * 8192, 16);
        qf = pack_pair(aq[0] * QS, aq[1] * QS);
        *(LAS u32x4*)(smem + L_Q + w * 1024 + lane * 16) = qf;
    }
    STAMP(12);

    const __amdgpu_buffer_rsrc_t rs_rg = __builtin_amdgcn_make_buffer_rsrc(ws + WS_RG, 0, 2048, 0x00020000);
    u32x4 rg0 = __builtin_amdgcn_raw_buffer_load_b128(rs_rg, lane * 32, 0, 16), rg1 = __builtin_amdgcn_raw_buffer_load_b128(rs_rg, lane * 32 + 16, 0, 16);
    const unsigned pat = ((lane & 11) == 0) ? 0x3f803f80u : 0u;
    u32x4 xv0, xv1;
#pragma unroll
    for (int j = 0; j < 4; ++j) { auto r = __builtin_amdgcn_permlane16_swap(vc[j], pat, false, false); xv0[j] = r[0]; xv1[j] = r[1]; }
    f32x16 o0 = {}, o1 = {}, o2 = {}, o3 = {};
    const int idx = 0xdddd;
    const char* kvf = ws + WS_KVF + (head * 4 + kq) * 128;
    lds_bar();
    const u32x4 q0 = qf;
    const u32x4 q1 = *(const LAS u32x4*)(smem + L_Q + (hh * 4 + ((kq + 1) & 3)) * 1024 + lane * 16);
    const u32x4 q2 = *(const LAS u32x4*)(smem + L_Q + (hh * 4 + ((kq + 2) & 3)) * 1024 + lane * 16);
    const u32x4 q3 = *(const LAS u32x4*)(smem + L_Q + (hh * 4 + ((kq + 3) & 3)) * 1024 + lane * 16);
    asm volatile(
        "s_nop 1\n\t" QKM(SA, "%[k]", "%[q0]") "s_nop 11\n\t" EX(192) EX(193)
        UNIT_A("%[o0]", "%[v0]", "%[v1]", QKM(SB, "%[k]", "%[q1]"))
        UNIT_B("%[o1]", "%[v0]", "%[v1]", QKM(SA, "%[k]", "%[q2]"))
        "s_waitcnt vmcnt(0)\n\t" FLAG("%[fo]", "%[fb]")
        UNIT_A("%[o2]", "%[v0]", "%[v1]", QKM(SB, "%[k]", "%[q3]"))
        UNIT_B_LAST("%[o3]", "%[v0]", "%[v1]") "s_nop 11"
        : [o0] "+v"(o0), [o1] "+v"(o1), [o2] "+v"(o2), [o3] "+v"(o3)
        : [k] "v"(kf), [v0] "v"(xv0), [v1] "v"(xv1), [q0] "v"(q0), [q1] "v"(q1), [q2] "v"(q2), [q3] "v"(q3), [idx] "v"(idx),
          [fo] "v"(tb * 8), [fb] "s"(kvf), [vtag] "v"(tag)
        : S_CLOB, "memory");
    STAMP(13);
    asm volatile("" :: "v"(pf));
    { const unsigned tl = (unsigned)tag, th = (unsigned)(tag >> 32) & 31u;
      for (;;) {
          const bool ok = rg0[0] == tl && rg0[2] == tl && rg1[0] == tl && rg1[2] == tl && (rg0[1] & 31u) == th && (rg0[3] & 31u) == th && (rg1[1] & 31u) == th && (rg1[3] & 31u) == th;
          if (__all(ok) || spin <= 0) break;
          --spin; __builtin_amdgcn_s_sleep(1);
          rg0 = __builtin_amdgcn_raw_buffer_load_b128(rs_rg, lane * 32, 0, 16); rg1 = __builtin_amdgcn_raw_buffer_load_b128(rs_rg, lane * 32 + 16, 0, 16);
      } }
    int sel; float rsel;
    { const unsigned k0 = max(max(rg0[1], rg0[3]), max(rg1[1], rg1[3])) >> 5;
      const unsigned kk = __builtin_amdgcn_readfirstlane(wave_maxu(k0));
      sel = 2047 - (int)(kk & 2047u);
      const unsigned mono = kk >> 11, rb = (mono & 0x8000u) ? (mono & 0x7fffu) : (~mono & 0xffffu);
      rsel = bfr(sigmoidf_(bflo(rb))); }
    if (blockIdx.x == 0 && tid == 0) __hip_atomic_store((GAS u64*)(ws + WS_EP), tag, RLX_AGENT);
    const unsigned xs = p.x[(size_t)sel * 512 + tid];
    const u32x4 wgv = *(const u32x4*)(p.wg + (size_t)tid * 1792 + 8 * fi), wuv = *(const u32x4*)(p.wu + (size_t)tid * 1792 + 8 * fi);
    unsigned wdv[8];
#pragma unroll
    for (int f = 0; f < 8; ++f) wdv[f] = p.wd[(size_t)(8 * fi + f) * 512 + tid];
    STAMP(14);
    int rdy = (int)(unsigned)poll_flags(kvf + t16 * 8, lane < 16, (1ull << ((tb + 1) & 15)) | (1ull << ((tb + 2) & 15)), tag, spin);
    STAMP(15);
    u32x4 yk, yv0, yv1, zk, zv0, zv1;
    yv0 = yv1 = zv0 = zv1 = (u32x4){pat, pat, pat, pat};
    { const int s1 = ((tb + 1) & 15) * 8192, s2 = ((tb + 2) & 15) * 8192;
      yk = __builtin_amdgcn_raw_buffer_load_b128(rs_kv, lane * 16, s1, 16);
      zk = __builtin_amdgcn_raw_buffer_load_b128(rs_kv, lane * 16, s2, 16);
      if ((lane & 16) == 0) {
          yv0 = __builtin_amdgcn_raw_buffer_load_b128(rs_kv, lane * 16 + 1024, s1, 16); yv1 = __builtin_amdgcn_raw_buffer_load_b128(rs_kv, lane * 16 + 1280, s1, 16);
          zv0 = __builtin_amdgcn_raw_buffer_load_b128(rs_kv, lane * 16 + 1024, s2, 16); zv1 = __builtin_amdgcn_raw_buffer_load_b128(rs_kv, lane * 16 + 1280, s2, 16); } }
    float y = 0.f;
    if (ffn) {
        const float xf = bflo(xs);
        float a[16];
#pragma unroll
        for (int j = 0; j < 4; ++j) { a[2 * j] = xf * bflo(wgv[j]); a[2 * j + 1] = xf * bfhi(wgv[j]); a[8 + 2 * j] = xf * bflo(wuv[j]); a[9 + 2 * j] = xf * bfhi(wuv[j]); }
#pragma unroll
        for (int j = 0; j < 8; ++j) { auto r = __builtin_amdgcn_permlane32_swap(__float_as_uint(a[j]), __float_as_uint(a[j + 8]), false, false); a[j] = __uint_as_float(r[0]) + __uint_as_float(r[1]); }
#pragma unroll
        for (int j = 0; j < 4; ++j) { auto r = __builtin_amdgcn_permlane16_swap(__float_as_uint(a[j]), __float_as_uint(a[j + 4]), false, false); a[j] = __uint_as_float(r[0]) + __uint_as_float(r[1]); }
        const f32x4 sm = {row_sum16(a[0]), row_sum16(a[1]), row_sum16(a[2]), row_sum16(a[3])};
        if (t16 == 0) *(LAS f32x4*)(smem + L_FS + w * 64 + g4 * 16) = sm;
        lds_bar();
        { float v = *(const LAS float*)(smem + L_FS + (lane & 7) * 64 + (lane & 8) * 4 + w * 4);
          v += dppf<0xb1>(v); v += dppf<0x4e>(v); v += dppf<0x141>(v);
          const float gs = bfr(v), us = bfr(dppf<0x128>(v));
          const float hv = bfr(bfr(gs * sigmoidf_(gs)) * us);
          if (lane == 0) *(LAS float*)(smem + L_FH + 4 * w) = hv; }
        lds_bar();
        { const f32x4 h0 = *(const LAS f32x4*)(smem + L_FH), h1 = *(const LAS f32x4*)(smem + L_FH + 16);

#pragma unroll
          for (int f = 0; f < 4; ++f) { y = fmaf(h0[f], bflo(wdv[f]), y); y = fmaf(h1[f], bflo(wdv[f + 4]), y); } }
    }
    asm volatile("" :: "v"(yk), "v"(yv0), "v"(yv1), "v"(zk), "v"(zv0), "v"(zv1) : "memory");
    if (ffn) __hip_atomic_store((GAS float*)(ws + WS_YP) + fi * 512 + tid, y, RLX_AGENT);
    { const char* wob = (const char*)p.wo + hp * 64;
#pragma unroll
      for (int i = 0; i < 4; ++i) {
          const int k = 64 * w + 16 * i + (lane >> 2);
          dma16(wob, k * 1024 + (((lane & 3) ^ (((k >> 2) & 1) << 1)) << 4), lds0 + L_WO + (4 * w + i) * 1024);
      } }
    STAMP(16);
    { int so = ((tb + 2) & 15) * 8192, cnt = 5, tmp;
      const u64 vm = 0x0000ffff0000ffffull;
      asm volatile(
          "s_nop 1\n\t" QKM(SA, "%[yk]", "%[q0]") "s_nop 11\n\t" EX(192) EX(193)
          "1:\n\t"
          TILE("%[yk]", "%[yv0]", "%[yv1]", "%[zk]", "%[xk]", "%[xv0]", "%[xv1]", "s_cmp_lg_u32 %[cnt], 5\n\ts_cbranch_scc1 4f\n\t" FLAG("%[yo]", "%[yb]") "4:\n\t")
          TILE("%[zk]", "%[zv0]", "%[zv1]", "%[xk]", "%[yk]", "%[yv0]", "%[yv1]", "")
          TILE("%[xk]", "%[xv0]", "%[xv1]", "%[yk]", "%[zk]", "%[zv0]", "%[zv1]", "")
          "s_sub_u32 %[cnt], %[cnt], 1\n\ts_cmp_lg_u32 %[cnt], 0\n\ts_cbranch_scc1 1b\n\t"
          "s_waitcnt vmcnt(0)\n\ts_nop 11"
          : [o0] "+v"(o0), [o1] "+v"(o1), [o2] "+v"(o2), [o3] "+v"(o3),
            [xk] "+v"(kf), [xv0] "+v"(xv0), [xv1] "+v"(xv1), [yk] "+v"(yk), [yv0] "+v"(yv0), [yv1] "+v"(yv1),
            [zk] "+v"(zk), [zv0] "+v"(zv0), [zv1] "+v"(zv1), [so] "+s"(so), [cnt] "+s"(cnt), [rdy] "+s"(rdy), [spin] "+s"(spin), [t] "=&s"(tmp)
          : [q0] "v"(q0), [q1] "v"(q1), [q2] "v"(q2), [q3] "v"(q3), [idx] "v"(idx), [ko] "v"(lane * 16),
            [rs] "s"(rs_kv), [vm] "s"(vm), [fo] "v"(t16 * 8), [fb] "s"(kvf), [tag] "s"(tag), [vtag] "v"(tag),
            [yo] "v"((ffn ? fi : 255) * 8), [yb] "s"(ws + WS_YF + w * 2048)
          : S_CLOB, "v224", "v225", "vcc", "scc", "memory"); }
    STAMP(17);
    { auto put = [&](const f32x16& o, int j) {
          LAS char* d = smem + L_CB + ((hh * 4 + ((kq + j) & 3)) * 3 + (j - 1)) * 2304;
          *(LAS f32x4*)(d + lane * 16) = (f32x4){o[0], o[1], o[2], o[3]};
          *(LAS f32x4*)(d + 1024 + lane * 16) = (f32x4){o[4], o[5], o[6], o[7]};
          *(LAS float*)(d + 2048 + lane * 4) = o[8]; };
      put(o1, 1); put(o2, 2); put(o3, 3); }
    lds_bar();
    STAMP(18);
    float o[9];
#pragma unroll
    for (int r = 0; r < 9; ++r) o[r] = o0[r];
#pragma unroll
    for (int j = 0; j < 3; ++j) {
        const LAS char* d = smem + L_CB + (w * 3 + j) * 2304;
        const f32x4 a = *(const LAS f32x4*)(d + lane * 16), b = *(const LAS f32x4*)(d + 1024 + lane * 16);
#pragma unroll
        for (int r = 0; r < 4; ++r) { o[r] += a[r]; o[4 + r] += b[r]; }
        o[8] += *(const LAS float*)(d + 2048 + lane * 4);
    }
    { float mag = 0.f;
#pragma unroll
      for (int r = 0; r < 8; ++r) mag += fabsf(o[r]);
      const bool safe = o[8] >= 9.313225746154785e-10f && o[8] <= 1.8446744073709552e19f && mag <= 3.0e38f;
      if (!__all(safe)) {
          f32x16 acc = {}; float m = -INFINITY;
          u32x4 v0 = {pat, pat, pat, pat}, v1 = v0;
          const __amdgpu_buffer_rsrc_t rs_all = __builtin_amdgcn_make_buffer_rsrc(ws + WS_KV + (size_t)head * 131072, 0, 131072, 0x00020000);
          for (int t = 0; t < 64; ++t) {
              const u32x4 kk = __builtin_amdgcn_raw_buffer_load_b128(rs_all, lane * 16, t * 2048, 16);
              if ((lane & 16) == 0) { v0 = __builtin_amdgcn_raw_buffer_load_b128(rs_all, lane * 16 + 1024, t * 2048, 16); v1 = __builtin_amdgcn_raw_buffer_load_b128(rs_all, lane * 16 + 1280, t * 2048, 16); }
              f32x16 s = __builtin_amdgcn_mfma_f32_32x32x16_bf16(__builtin_bit_cast(bf16x8, kk), __builtin_bit_cast(bf16x8, q0), (f32x16){}, 0, 0, 0);
              float tm = s[0];
#pragma unroll
              for (int r = 1; r < 16; ++r) tm = fmaxf(tm, s[r]);
              auto sw = __builtin_amdgcn_permlane32_swap(__float_as_uint(tm), __float_as_uint(tm), false, false);
              const float mn = fmaxf(m, fmaxf(__uint_as_float(sw[0]), __uint_as_float(sw[1])));
              const float al = __builtin_amdgcn_exp2f(m - mn); m = mn;
#pragma unroll
              for (int r = 0; r < 16; ++r) { s[r] = __builtin_amdgcn_exp2f(s[r] - mn); acc[r] *= al; }
              acc = __builtin_amdgcn_smfmac_f32_32x32x32_bf16(__builtin_bit_cast(bf16x8, v0), __builtin_bit_cast(bf16x16, __builtin_shufflevector(s, s, 0, 1, 2, 3, 4, 5, 6, 7)), acc, idx, 0, 0);
              acc = __builtin_amdgcn_smfmac_f32_32x32x32_bf16(__builtin_bit_cast(bf16x8, v1), __builtin_bit_cast(bf16x16, __builtin_shufflevector(s, s, 8, 9, 10, 11, 12, 13, 14, 15)), acc, idx, 0, 0);
          }
#pragma unroll
          for (int r = 0; r < 9; ++r) o[r] = acc[r];
      } }
    { const float inv = __builtin_amdgcn_rcpf(o[8]);
      const u32x4 of = {cvtpk(o[0] * inv, o[1] * inv), cvtpk(o[2] * inv, o[3] * inv), cvtpk(o[4] * inv, o[5] * inv), cvtpk(o[6] * inv, o[7] * inv)};
      const __amdgpu_buffer_rsrc_t rs_o = __builtin_amdgcn_make_buffer_rsrc(ws + WS_O, 0, 2u << 20, 0x00020000);
      __builtin_amdgcn_raw_buffer_store_b128(of, rs_o, lane * 16, (4 * tb + kq) * 32768 + head * 1024, 16);
      asm volatile("s_waitcnt vmcnt(0)" ::: "memory");
      if (lane == 0) __hip_atomic_store((GAS u64*)(ws + WS_OF) + (4 * tb + kq) * 32 + head, tag, RLX_AGENT); }
    STAMP(19);

    const int tok = 128 * tb + 16 * w + t16;
    const u32x2 xa = *(const u32x2*)(p.x + (size_t)tok * 512 + 32 * hp + 4 * g4), xc = *(const u32x2*)(p.x + (size_t)tok * 512 + 32 * hp + 16 + 4 * g4);
    u32x4 wf[32];
    { const LAS char* wa = smem + L_WO + ((g4 >> 1) * 16 + (g4 & 1) * 4 + ((lane & 15) >> 2)) * 64 + (lane & 3) * 8;
      const int h0 = (g4 & 1) << 5, h1 = h0 ^ 32;
#pragma unroll
      for (int kk = 0; kk < 16; ++kk) { wf[2 * kk] = trd2(wa + kk * 2048 + h0, 512); wf[2 * kk + 1] = trd2(wa + kk * 2048 + h1, 512); } }
    const bool own = (sel >> 7) == tb;
    if (own) {
        poll_flags(ws + WS_YF + (hp >> 1) * 2048 + (28 * w + min(lane, 27)) * 8, true, ~0ull, tag, spin);
        const __amdgpu_buffer_rsrc_t rs_y = __builtin_amdgcn_make_buffer_rsrc(ws + WS_YP + (size_t)w * 28 * 2048, 0, 28 * 2048, 0x00020000);
        f32x4 s = {};
#pragma unroll
        for (int i = 0; i < 4; ++i) s += __builtin_bit_cast(f32x4, __builtin_amdgcn_raw_buffer_load_b128(rs_y, (lane >> 3) * 2048 + hp * 128 + (lane & 7) * 16, i * 16384, 16));
#pragma unroll
        for (int r = 0; r < 4; ++r) {
            float v = s[r]; v += dppf<0x128>(v);
            auto a = __builtin_amdgcn_permlane16_swap(__float_as_uint(v), __float_as_uint(v), false, false); v = __uint_as_float(a[0]) + __uint_as_float(a[1]);
            auto b = __builtin_amdgcn_permlane32_swap(__float_as_uint(v), __float_as_uint(v), false, false); s[r] = __uint_as_float(b[0]) + __uint_as_float(b[1]); }
        if (lane < 8) *(LAS f32x4*)(smem + L_YS + w * 128 + lane * 16) = s;
        lds_bar();
    }
#pragma unroll
    for (int i = 0; i < 32; ++i) asm volatile("" : "+v"(wf[i]));
    STAMP(20);
    poll_flags(ws + WS_OF + ((4 * tb + (w >> 1)) * 32 + (lane & 31)) * 8, true, ~0ull, tag, spin);
    STAMP(21);
    const __amdgpu_buffer_rsrc_t rs_o = __builtin_amdgcn_make_buffer_rsrc(ws + WS_O + (size_t)(4 * tb + (w >> 1)) * 32768, 0, 32768, 0x00020000);
    u32x4 ob[16];
    { const int oo = (g4 >> 1) * 1024 + ((g4 & 1) * 32 + 16 * (w & 1) + t16) * 16;
#pragma unroll
      for (int kk = 0; kk < 16; ++kk) ob[kk] = __builtin_amdgcn_raw_buffer_load_b128(rs_o, oo, kk * 2048, 16); }
    const float gt = __hip_atomic_load((GAS float*)(ws + WS_G) + tok, RLX_AGENT);
    __builtin_amdgcn_sched_barrier(0);
    f32x4 c0 = {}, c1 = {};
#pragma unroll
    for (int kk = 0; kk < 16; ++kk) { c0 = mfma16(wf[2 * kk], ob[kk], c0); c1 = mfma16(wf[2 * kk + 1], ob[kk], c1); }
    float bb[8] = {bflo(xa[0]), bfhi(xa[0]), bflo(xa[1]), bfhi(xa[1]), bflo(xc[0]), bfhi(xc[0]), bflo(xc[1]), bfhi(xc[1])};
    if (own && tok == sel) {
        f32x4 y0 = {}, y1 = {};
#pragma unroll
        for (int i = 0; i < 8; ++i) { y0 += *(const LAS f32x4*)(smem + L_YS + i * 128 + g4 * 16); y1 += *(const LAS f32x4*)(smem + L_YS + i * 128 + 64 + g4 * 16); }
#pragma unroll
        for (int r = 0; r < 4; ++r) { bb[r] = bfr(bb[r] + bfr(bfr(y0[r]) * rsel)); bb[4 + r] = bfr(bb[4 + r] + bfr(bfr(y1[r]) * rsel)); }
    }
    const float gm = bfr(1.0f - gt);
    f32x4 r0, r1;
#pragma unroll
    for (int r = 0; r < 4; ++r) { r0[r] = bfr(gt * bfr(c0[r])) + bfr(gm * bb[r]); r1[r] = bfr(gt * bfr(c1[r])) + bfr(gm * bb[4 + r]); }
    *(u32x4*)(p.out + (size_t)tok * 512 + 32 * hp + 16 * (g4 & 1) + 8 * (g4 >> 1)) = pack_pair(r0, r1);
}

extern "C" void kernel_launch(void* const* d_in, const int* in_sizes, int n_in,
                              void* d_out, int out_size, void* d_ws, size_t ws_size,
                              hipStream_t stream) {
    Params p;
    p.x = (const uint16_t*)d_in[0]; p.wqkv = (const uint16_t*)d_in[1]; p.wo = (const uint16_t*)d_in[2];
    p.wr = (const uint16_t*)d_in[4]; p.wg = (const uint16_t*)d_in[5]; p.wu = (const uint16_t*)d_in[6];
    p.wd = (const uint16_t*)d_in[7]; p.gw = (const uint16_t*)d_in[8];
    p.out = (uint16_t*)d_out; p.ws = (char*)d_ws;
    mix_kernel<<<256, 512, 0, stream>>>(p);
}
```

```cpp
#include <hip/hip_runtime.h>
#include <hip/hip_bf16.h>
#include <stdint.h>

typedef __attribute__((ext_vector_type(8))) __bf16 bf16x8;
typedef __attribute__((ext_vector_type(16))) __bf16 bf16x16;
typedef __attribute__((ext_vector_type(4))) float f32x4;
typedef __attribute__((ext_vector_type(8))) float f32x8;
typedef __attribute__((ext_vector_type(16))) float f32x16;
typedef __attribute__((ext_vector_type(4))) unsigned u32x4;
typedef __attribute__((ext_vector_type(2))) unsigned u32x2;
typedef __attribute__((ext_vector_type(4))) short s16x4;
typedef unsigned long long u64;
#define LAS __attribute__((address_space(3)))
#define GAS __attribute__((address_space(1)))
#define RLX_AGENT __ATOMIC_RELAXED, __HIP_MEMORY_SCOPE_AGENT

constexpr size_t WS_KV = 0;
constexpr size_t WS_O = 4u << 20;
constexpr size_t WS_YP = 6u << 20;
constexpr size_t WS_G = 7u << 20;
constexpr size_t WS_RG = WS_G + 16384;
constexpr size_t WS_KVF = WS_RG + 2048;
constexpr size_t WS_OF = WS_KVF + 32768;
constexpr size_t WS_P1 = WS_OF + 32768;
constexpr size_t WS_EP = WS_P1 + 229376;

constexpr int SLOT = 28672, NSLOT = 5;
constexpr int L_MISC = SLOT * NSLOT;
constexpr int L_RK = L_MISC;
constexpr int L_KM = L_MISC + 32;
constexpr int L_FS = 2 * SLOT;
constexpr int L_YS = L_MISC + 64;
constexpr int L_Q = L_MISC + 2048;
constexpr int L_RW = L_Q + 8192;
constexpr int LDS_BYTES = L_RW + 2048;
constexpr int L_WO = 3 * SLOT;
constexpr int L_CB = 0;
constexpr int SPIN_CAP = 200000;
#define WIN 3
#define RPOS 2
#define CODE_LINES 190
#define ROT 4
#define DSTEP 3
#define DOFF 0

#define STAMP(i)
#define CSTAMP(i)
#define STAMP_DECL
#define STAMP_OUT

struct Params {
    const uint16_t* x; const uint16_t* wqkv; const uint16_t* wo; const uint16_t* wr;
    const uint16_t* wg; const uint16_t* wu; const uint16_t* wd; const uint16_t* gw;
    uint16_t* out; char* ws;
};

__device__ __forceinline__ float bflo(unsigned u) { return __uint_as_float(u << 16); }
__device__ __forceinline__ float bfhi(unsigned u) { return __uint_as_float(u & 0xffff0000u); }
__device__ __forceinline__ unsigned cvtpk(float lo, float hi) {
    typedef __attribute__((ext_vector_type(2))) float f2; typedef __attribute__((ext_vector_type(2))) __bf16 b2;
    return __builtin_bit_cast(unsigned, __builtin_convertvector((f2){lo, hi}, b2));
}
__device__ __forceinline__ float bfr(float v) { return bflo(cvtpk(v, 0.f) & 0xffffu); }
__device__ __forceinline__ float sigmoidf_(float v) { return __builtin_amdgcn_rcpf(1.0f + __builtin_amdgcn_exp2f(v * -1.4426950408889634f)); }
__device__ __forceinline__ f32x4 mfma16(u32x4 a, u32x4 b, f32x4 c) {
    return __builtin_amdgcn_mfma_f32_16x16x32_bf16(__builtin_bit_cast(bf16x8, a), __builtin_bit_cast(bf16x8, b), c, 0, 0, 0);
}
__device__ __forceinline__ u32x2 trd(const LAS char* p) {
    return __builtin_bit_cast(u32x2, __builtin_amdgcn_ds_read_tr16_b64_v4i16((LAS s16x4*)p));
}
__device__ __forceinline__ u32x4 trd2(const LAS char* p, int d) { u32x2 a = trd(p), b = trd(p + d); return (u32x4){a[0], a[1], b[0], b[1]}; }
__device__ __forceinline__ void dma16(const void* sbase, unsigned voff, unsigned lds) {
    unsigned keep;
    asm volatile("s_mov_b32 %0, m0\n\ts_mov_b32 m0, %3\n\ts_nop 0\n\tglobal_load_lds_dwordx4 %1, %2\n\ts_mov_b32 m0, %0"
                 : "=&s"(keep) : "v"(voff), "s"(sbase), "s"(lds) : "memory");
}
__device__ __forceinline__ void dma16_half(const void* sbase, unsigned voff, unsigned lds) {
    unsigned keep;
    asm volatile("s_mov_b32 %0, m0\n\ts_mov_b32 m0, %3\n\ts_mov_b32 exec_hi, 0\n\tglobal_load_lds_dwordx4 %1, %2\n\ts_mov_b32 exec_hi, -1\n\ts_mov_b32 m0, %0"
                 : "=&s"(keep) : "v"(voff), "s"(sbase), "s"(lds) : "memory");
}
template <int N> __device__ __forceinline__ void wait_bar() {
    asm volatile("" ::: "memory"); __builtin_amdgcn_s_waitcnt(0xc07f);
    asm volatile("s_waitcnt vmcnt(%0) lgkmcnt(0)\n\ts_barrier" :: "n"(N) : "memory"); }
template <int N> __device__ __forceinline__ void vm_bar() { asm volatile("s_waitcnt vmcnt(%0)\n\ts_barrier" :: "n"(N) : "memory"); }
__device__ __forceinline__ void lds_bar() { asm volatile("s_waitcnt lgkmcnt(0)\n\ts_barrier" ::: "memory"); }
__device__ __forceinline__ void bare_bar() { asm volatile("s_barrier" ::: "memory"); }
template <int CTRL> __device__ __forceinline__ float dppf(float v) {
    return __int_as_float(__builtin_amdgcn_update_dpp(0, __float_as_int(v), CTRL, 0xf, 0xf, true));
}
template <int CTRL> __device__ __forceinline__ unsigned dppu(unsigned v) {
    return (unsigned)__builtin_amdgcn_update_dpp(0, (int)v, CTRL, 0xf, 0xf, true);
}
__device__ __forceinline__ float row_sum16(float v) {
    v += dppf<0x128>(v); v += dppf<0x124>(v); v += dppf<0x122>(v); v += dppf<0x121>(v); return v;
}
__device__ __forceinline__ float wave_sum(float v) {
    v = row_sum16(v);
    auto a = __builtin_amdgcn_permlane16_swap(__float_as_uint(v), __float_as_uint(v), false, false);
    v = __uint_as_float(a[0]) + __uint_as_float(a[1]);
    auto b = __builtin_amdgcn_permlane32_swap(__float_as_uint(v), __float_as_uint(v), false, false);
    return __uint_as_float(b[0]) + __uint_as_float(b[1]);
}
__device__ __forceinline__ unsigned wave_maxu(unsigned v) {
    v = max(v, dppu<0x128>(v)); v = max(v, dppu<0x124>(v)); v = max(v, dppu<0x122>(v)); v = max(v, dppu<0x121>(v));
    auto a = __builtin_amdgcn_permlane16_swap(v, v, false, false); v = max(a[0], a[1]);
    auto b = __builtin_amdgcn_permlane32_swap(v, v, false, false); return max(b[0], b[1]);
}
__device__ __forceinline__ u32x4 pack_pair(f32x4 a, f32x4 b) {
    auto x = __builtin_amdgcn_permlane16_swap(cvtpk(a[0], a[1]), cvtpk(b[0], b[1]), false, false);
    auto y = __builtin_amdgcn_permlane16_swap(cvtpk(a[2], a[3]), cvtpk(b[2], b[3]), false, false);
    return (u32x4){x[0], y[0], x[1], y[1]};
}
#define STR2(x) #x
#define STR(x) STR2(x)
#define EX(r) "v_exp_f32 v" STR(r) ", v" STR(r) "\n\t"
#define QKM(S, K, Q) "v_mfma_f32_32x32x16_bf16 " S ", " K ", " Q ", 0\n\t"
#define PVM(O, V, P) "v_smfmac_f32_32x32x32_bf16 " O ", " V ", " P ", %[idx]\n\t"
#define UNIT_A(O, V0, V1, MID) EX(194) EX(195) EX(196) MID EX(197) EX(198) EX(199) EX(200) EX(201) PVM(O, V0, "v[192:199]") \
    EX(202) EX(203) EX(204) EX(205) EX(206) EX(207) EX(208) EX(209) PVM(O, V1, "v[200:207]")
#define UNIT_B(O, V0, V1, MID) EX(210) EX(211) EX(212) MID EX(213) EX(214) EX(215) EX(216) EX(217) PVM(O, V0, "v[208:215]") \
    EX(218) EX(219) EX(220) EX(221) EX(222) EX(223) EX(192) EX(193) PVM(O, V1, "v[216:223]")
#define UNIT_B_LAST(O, V0, V1) EX(210) EX(211) EX(212) EX(213) EX(214) EX(215) EX(216) EX(217) PVM(O, V0, "v[208:215]") \
    EX(218) EX(219) EX(220) EX(221) EX(222) EX(223) "s_nop 1\n\t" PVM(O, V1, "v[216:223]")
#define SA "v[192:207]"
#define SB "v[208:223]"
#define S_CLOB "v192", "v193", "v194", "v195", "v196", "v197", "v198", "v199", "v200", "v201", "v202", "v203", "v204", "v205", "v206", "v207", \
    "v208", "v209", "v210", "v211", "v212", "v213", "v214", "v215", "v216", "v217", "v218", "v219", "v220", "v221", "v222", "v223"
#define LOADT(J, K, V0, V1) "s_xor_b32 %[t], %[sb], " J "\n\t" \
    "buffer_load_dwordx4 " K ", %[ko], %[rs], %[t] offen sc1\n\t" \
    "s_mov_b64 exec, %[vm]\n\t" \
    "buffer_load_dwordx4 " V0 ", %[ko], %[rs], %[t] offen offset:1024 sc1\n\t" \
    "buffer_load_dwordx4 " V1 ", %[ko], %[rs], %[t] offen offset:1280 sc1\n\t" \
    "s_mov_b64 exec, -1\n\t"
#define GROUP "s_add_u32 %[gi], %[gi], 0x8000\n\ts_and_b32 %[gi], %[gi], 0x18000\n\ts_xor_b32 %[sb], %[so], %[gi]\n\t" \
    "s_lshr_b32 %[t], %[sb], 13\n\ts_and_b32 %[t], %[t], 12\n\ts_lshl_b32 %[t], 15, %[t]\n\t" \
    "s_andn2_b32 %[u], %[t], %[rdy]\n\ts_cbranch_scc0 3f\n\t" \
    "2:\n\ts_waitcnt vmcnt(0)\n\t" \
    "v_cmp_eq_u64 vcc, %[tag], %[fl]\n\ts_nop 4\n\ts_and_b32 %[rdy], vcc_lo, 0xffff\n\t" \
    "s_andn2_b32 %[u], %[t], %[rdy]\n\ts_cbranch_scc0 3f\n\t" \
    "global_load_dwordx2 %[fl], %[fo], %[fb] sc1\n\t" \
    "s_sub_i32 %[spin], %[spin], 1\n\ts_cmp_gt_i32 %[spin], 0\n\ts_cbranch_scc1 2b\n\t" \
    "3:\n\t"
#define TILE(B, BN, LD) \
    UNIT_A("%[o0]", "%[" B "v0]", "%[" B "v1]", QKM(SB, "%[" B "k]", "%[q1]") LD) \
    UNIT_B("%[o1]", "%[" B "v0]", "%[" B "v1]", QKM(SA, "%[" B "k]", "%[q2]")) \
    UNIT_A("%[o2]", "%[" B "v0]", "%[" B "v1]", QKM(SB, "%[" B "k]", "%[q3]")) \
    UNIT_B("%[o3]", "%[" B "v0]", "%[" B "v1]", "s_waitcnt vmcnt(6)\n\t" QKM(SA, "%[" BN "k]", "%[q0]"))
#define WODMA "s_mov_b32 m0, %[wl]\n\ts_add_u32 %[wl], %[wl], 0x400\n\tglobal_load_lds_dwordx4 %[wv], %[wp]\n\tv_add_u32 %[wv], 0x4000, %[wv]\n\t"
#define LOADB(J, B) LOADT(J, "%[" B "k]", "%[" B "v0]", "%[" B "v1]")

__global__ __launch_bounds__(512, 2) void mix_kernel(Params p) {
    __shared__ __attribute__((aligned(16))) char smem_[LDS_BYTES];
    LAS char* smem = (LAS char*)smem_;
    const unsigned lds0 = (unsigned)(uintptr_t)smem;
    const int tid = threadIdx.x, lane = tid & 63;
    const int w = __builtin_amdgcn_readfirstlane(tid >> 6);
    const int xcd = blockIdx.x & 7, jj = blockIdx.x >> 3;
    const int tb = 4 * (xcd >> 1) + (jj & 3), hp = 8 * (xcd & 1) + (jj >> 2);
    const int kq = w & 3, hh = w >> 2, head = 2 * hp + hh;
    const int g4 = lane >> 4, t16 = lane & 15;
    char* const ws = p.ws;
    const __amdgpu_buffer_rsrc_t rs_kv = __builtin_amdgcn_make_buffer_rsrc(ws + WS_KV + (size_t)head * 131072 + kq * 2048, 0, 131072 - kq * 2048, 0x00020000);
    int spin = SPIN_CAP;
    STAMP_DECL; STAMP(0);

    const int tokr = 128 * tb + 8 * hp + w;
    u64 ep;
    asm volatile("s_load_dwordx2 %0, %1, 0x0" : "=s"(ep) : "s"(ws + WS_EP) : "memory");
    if (w < 2) dma16(w ? p.gw : p.wr, lane * 16, lds0 + L_RW + w * 1024);
    u32x4 rx;
    asm volatile("global_load_dwordx4 %0, %1, %2" : "=v"(rx) : "v"(lane * 16), "s"(p.x + (size_t)tokr * 512) : "memory");
    unsigned xo0, xo1, wo0, wo1;
    { const int t0 = 16 * w + (lane >> 3), t1 = t0 + 8;
      xo0 = t0 * 1024 + (((lane & 7) ^ ((t0 >> 1) & 7)) << 4);
      xo1 = t1 * 1024 + (((lane & 7) ^ ((t1 >> 1) & 7)) << 4);
      const int k0 = 16 * (w & 3) + (lane >> 2), pc = ((lane & 3) ^ (((k0 >> 3) & 1) << 1)) << 4;
      wo0 = k0 * 3072 + (w >> 2) * 1024 + pc;
      const int k1 = 8 * w + ((lane >> 2) & 7);
      wo1 = k1 * 3072 + 2048 + (((lane & 3) ^ ((w & 1) << 1)) << 4); }
    const char* xb = (const char*)p.x + (size_t)tb * 131072;
    const char* wb = (const char*)p.wqkv + hp * 64;
    const int rot = ROT == 0 ? 0 : ROT == 1 ? (2 * (jj & 3) + (jj >> 2)) & 7 : ROT == 2 ? (((jj & 3) + (jj >> 2)) & 1) * 4 : ((jj & 3) + (jj >> 2) * 2 + (jj >> 3)) & 3;
    auto issue = [&](int c, int i) {
        const unsigned s = lds0 + (c % NSLOT) * SLOT; const int cc = ROT == 4 ? c ^ rot : (c + rot) & 7;
        if (i == 0) dma16(xb + cc * 128, xo0, s + w * 2048);
        if (i == 1) dma16(wb + cc * 196608, wo0, s + 16384 + w * 1024);
        if (i == 2) dma16(xb + cc * 128, xo1, s + w * 2048 + 1024);
        if (i == 3) dma16_half(wb + cc * 196608, wo1, s + 16384 + 8192 + w * 512);
    };
    const __amdgpu_buffer_rsrc_t rs_os = __builtin_amdgcn_make_buffer_rsrc(ws + WS_O + (size_t)(4 * tb + kq) * 32768 + head * 1024, 0, 1024, 0x00020000);
#pragma unroll
    for (int i = 0; i < 4; ++i) issue(0, i);
    if (tid == 0) *(LAS u32x4*)(smem + L_KM) = (u32x4){0u, 0u, 0u, 0u};
    asm volatile("s_waitcnt vmcnt(4) lgkmcnt(0)\n\ts_barrier" : "+v"(rx), "+s"(ep) :: "memory");
    const u64 tag = ep + 1;
    unsigned gbits, pf;
    {
            const u32x4 rw = *(const LAS u32x4*)(smem + L_RW + lane * 16), rgw = *(const LAS u32x4*)(smem + L_RW + 1024 + lane * 16);
            float ra = 0.f, ga = 0.f;
#pragma unroll
            for (int j = 0; j < 4; ++j) {
                const float a = bflo(rx[j]), b = bfhi(rx[j]);
                ra = fmaf(a, bflo(rw[j]), ra); ra = fmaf(b, bfhi(rw[j]), ra);
                const unsigned pr = cvtpk(a * bflo(rgw[j]), b * bfhi(rgw[j]));
                ga += bflo(pr); ga += bfhi(pr); }
            ra = wave_sum(ra); ga = wave_sum(ga);
            const unsigned rb = cvtpk(ra, 0.f) & 0xffffu;
            const unsigned key = (((rb & 0x8000u) ? (~rb & 0xffffu) : (rb | 0x8000u)) << 11) | (unsigned)(2047 - tokr);
            gbits = cvtpk(sigmoidf_(bfr(ga)), 0.f) & 0xffffu;
            if (lane == 0) *(LAS unsigned*)(smem + L_RK + 4 * w) = key; }
#pragma unroll
    for (int c = 1; c < WIN; ++c) {
#pragma unroll
        for (int i = 0; i < 4; ++i) issue(c, i);
        if (c == 1) { lds_bar();
            if (w == 0) {
                const unsigned kk = wave_maxu(*(const LAS unsigned*)(smem + L_RK + 4 * (lane & 7)));
                if (lane == 0) __hip_atomic_store((GAS u64*)(ws + WS_RG) + blockIdx.x, ((u64)kk << 37) | (tag & ((1ull << 37) - 1)), RLX_AGENT);
            }
        } else if (c + 1 < WIN) bare_bar();
    }
    const int fi = min((int)blockIdx.x, 223); const bool ffn = blockIdx.x < 224;
    const int frow = 64 * (fi & 7) + 8 * w + (lane >> 3);
    u32x4 wgv, wuv;
    const __amdgpu_buffer_rsrc_t rs_rg = __builtin_amdgcn_make_buffer_rsrc(ws + WS_RG, 0, 2048, 0x00020000);
    STAMP(1);
    u32x4 kf, vc, qf;
    {   f32x4 aq[2] = {}, ak[2] = {}, av[2] = {};
        const int fsw = (t16 >> 1) & 7;
        const int xa = (32 * kq + t16) * 128 + ((g4 ^ fsw) << 4);
        const int wa = 16384 + g4 * 512 + ((lane & 15) >> 2) * 64 + ((hh ^ (g4 & 1)) << 5) + (lane & 3) * 8;
        u32x4 fx[2][4], fw[2][6];
        auto frag = [&](int c, int i) {
            const LAS char* s = smem + (c % NSLOT) * SLOT; const int kk = i / 5, j = i % 5;
            if (j == 1 || j == 2) fx[c & 1][2 * kk + j - 1] = *(const LAS u32x4*)(s + (xa ^ (kk * 64)) + (j - 1) * 2048);
            else { const int m = j ? j - 2 : 0; fw[c & 1][3 * kk + m] = trd2(s + wa + m * 4096 + kk * 2048, 256); } };
        wait_bar<4 * (WIN - 1)>();
        STAMP(3); CSTAMP(24);
#pragma unroll
        for (int i = 0; i < 10; ++i) frag(0, i);
#pragma unroll
        for (int c = 0; c < 8; ++c) {
            if (c < 7) {
                const int last = c + WIN - 1 < 7 ? c + WIN - 1 : 7;
                switch (4 * (last - c - 1) + (c > 7 - WIN ? 3 : 0)) { case 4: vm_bar<4>(); break; case 8: vm_bar<8>(); break; case 7: vm_bar<7>(); break;
                    case 11: vm_bar<11>(); break; default: vm_bar<3>(); } }
            if (c == 1) { STAMP(6); CSTAMP(25); }
            if (c == 2) { STAMP(11); CSTAMP(26); }
            if (c == 3) { STAMP(7); CSTAMP(27); }
            if (c == 5) { STAMP(9); CSTAMP(28); }
            if (c == 6) { STAMP(10); }
            __builtin_amdgcn_sched_barrier(0);
#pragma unroll
            for (int i = 0; i < 12; ++i) {
                const int kk = i / 6, j = i % 6; const u32x4 xx = fx[c & 1][2 * kk + (j & 1)], ww = fw[c & 1][3 * kk + (j >> 1)];
                if (j < 2) aq[j] = mfma16(ww, xx, aq[j]); else if (j < 4) ak[j - 2] = mfma16(ww, xx, ak[j - 2]); else av[j - 4] = mfma16(xx, ww, av[j - 4]);
                if (c < 7 && i < 5) { frag(c + 1, 2 * i); frag(c + 1, 2 * i + 1); }
                if (c + WIN < 8 && i % DSTEP == DOFF && i / DSTEP < 4) issue(c + WIN, i / DSTEP);
                __builtin_amdgcn_sched_barrier(0);
            }
            if (c == 7 - WIN) {
                wgv = *(const u32x4*)(p.wg + (size_t)frow * 1792 + 64 * (fi >> 3) + 8 * (lane & 7));
                wuv = *(const u32x4*)(p.wu + (size_t)frow * 1792 + 64 * (fi >> 3) + 8 * (lane & 7));
                asm volatile("global_load_dword %0, %1, %2" : "=v"(pf) : "v"((jj * 128 + w * 16 + t16) * 128), "s"(p.wo) : "memory"); }
        }
        constexpr float QS = 0.25f * 1.4426950408889634f;
        kf = pack_pair(ak[0], ak[1]); vc = pack_pair(av[0], av[1]);
        __builtin_amdgcn_raw_buffer_store_b128(kf, rs_kv, lane * 16, tb * 8192, 16);
        __builtin_amdgcn_raw_buffer_store_b128(vc, rs_kv, lane * 16 + 1024, tb * 8192, 16);
        __builtin_amdgcn_sched_barrier(0);
        qf = pack_pair(aq[0] * QS, aq[1] * QS);
        *(LAS u32x4*)(smem + L_Q + w * 1024 + lane * 16) = qf;
        __builtin_amdgcn_sched_barrier(0);
    }
    STAMP(12); CSTAMP(29);

    const unsigned pat = ((lane & 11) == 0) ? 0x3f803f80u : 0u;
    u32x4 xv0, xv1;
#pragma unroll
    for (int j = 0; j < 4; ++j) { auto r = __builtin_amdgcn_permlane16_swap(vc[j], pat, false, false); xv0[j] = r[0]; xv1[j] = r[1]; }
    f32x16 o0 = {}, o1 = {}, o2 = {}, o3 = {};
    const int idx = 0xdddd;
    const char* kvf = ws + WS_KVF + hp * 2048;
    lds_bar();
    const u32x4 q0 = qf;
    const u32x4 q1 = *(const LAS u32x4*)(smem + L_Q + (hh * 4 + ((kq + 1) & 3)) * 1024 + lane * 16);
    const u32x4 q2 = *(const LAS u32x4*)(smem + L_Q + (hh * 4 + ((kq + 2) & 3)) * 1024 + lane * 16);
    const u32x4 q3 = *(const LAS u32x4*)(smem + L_Q + (hh * 4 + ((kq + 3) & 3)) * 1024 + lane * 16);
    auto own_pair = [&](f32x16& oa, f32x16& ob_, const u32x4& qa, const u32x4& qb) {
        asm volatile(
            "s_nop 1\n\t" QKM(SA, "%[k]", "%[q0]") "s_nop 11\n\t" EX(192) EX(193)
            UNIT_A("%[o0]", "%[v0]", "%[v1]", QKM(SB, "%[k]", "%[q1]"))
            UNIT_B_LAST("%[o1]", "%[v0]", "%[v1]") "s_nop 11"
            : [o0] "+v"(oa), [o1] "+v"(ob_)
            : [k] "v"(kf), [v0] "v"(xv0), [v1] "v"(xv1), [q0] "v"(qa), [q1] "v"(qb), [idx] "v"(idx)
            : S_CLOB); };
    own_pair(o0, o1, q0, q1);
    STAMP(13);
    asm volatile("s_waitcnt vmcnt(0)\n\ts_barrier" :: "v"(pf) : "memory");
    if (tid == 0) __hip_atomic_store((GAS u64*)(kvf + tb * 128), tag, RLX_AGENT);
    u32x4 rg0 = __builtin_amdgcn_raw_buffer_load_b128(rs_rg, lane * 32, 0, 16), rg1 = __builtin_amdgcn_raw_buffer_load_b128(rs_rg, lane * 32 + 16, 0, 16);
    __builtin_amdgcn_sched_barrier(0);
    own_pair(o2, o3, q2, q3);
    asm volatile("" : "+v"(rg0), "+v"(rg1));
    STAMP(15);
    { const unsigned tl = (unsigned)tag, th = (unsigned)(tag >> 32) & 31u;
      auto fresh = [&]() { return __all((((rg0[0] ^ tl) | (rg0[2] ^ tl) | (rg1[0] ^ tl) | (rg1[2] ^ tl)) | (((rg0[1] ^ th) | (rg0[3] ^ th) | (rg1[1] ^ th) | (rg1[3] ^ th)) & 31u)) == 0); };
      if (!fresh()) do {
          --spin; asm volatile("" ::: "memory");
          rg0 = __builtin_amdgcn_raw_buffer_load_b128(rs_rg, lane * 32, 0, 16); rg1 = __builtin_amdgcn_raw_buffer_load_b128(rs_rg, lane * 32 + 16, 0, 16);
      } while (!fresh() && spin > 0); }
    int sel; float rsel;
    { const unsigned k0 = max(max(rg0[1], rg0[3]), max(rg1[1], rg1[3])) >> 5;
      const unsigned kk = __builtin_amdgcn_readfirstlane(wave_maxu(k0));
      sel = 2047 - (int)(kk & 2047u);
      const unsigned mono = kk >> 11, rb = (mono & 0x8000u) ? (mono & 0x7fffu) : (~mono & 0xffffu);
      rsel = bfr(sigmoidf_(bflo(rb))); }
    const unsigned xs = p.x[(size_t)sel * 512 + frow];
    if (blockIdx.x == 0 && tid == 0) __hip_atomic_store((GAS u64*)(ws + WS_EP), tag, RLX_AGENT);
    STAMP(14);

    volatile LAS unsigned* km = (volatile LAS unsigned*)(smem + L_KM);
    { const unsigned need = (15u << (tb & 12)) & ~(1u << tb);
      for (;;) {
          unsigned m;
          if (w == 0) { m = (unsigned)__ballot(__hip_atomic_load((GAS u64*)(kvf + t16 * 128), RLX_AGENT) == tag) & 0xffffu; km[0] = m; }
          else { m = __builtin_amdgcn_readfirstlane(km[0]); __builtin_amdgcn_s_sleep(1); }
          if ((m & need) == need || spin <= 0) break;
          --spin; }
      asm volatile("" :: "v"(xs), "v"(wgv), "v"(wuv) : "memory"); }
    STAMP(2);
    u32x4 yk, yv0, yv1, zk, zv0, zv1, uk, uv0, uv1;
    yv0 = yv1 = zv0 = zv1 = uv0 = uv1 = (u32x4){pat, pat, pat, pat};
    { const int s1 = (tb ^ 1) * 8192, s2 = (tb ^ 2) * 8192, s3 = (tb ^ 3) * 8192;
      yk = __builtin_amdgcn_raw_buffer_load_b128(rs_kv, lane * 16, s1, 16);
      if ((lane & 16) == 0) {
          yv0 = __builtin_amdgcn_raw_buffer_load_b128(rs_kv, lane * 16 + 1024, s1, 16); yv1 = __builtin_amdgcn_raw_buffer_load_b128(rs_kv, lane * 16 + 1280, s1, 16);
          zv0 = __builtin_amdgcn_raw_buffer_load_b128(rs_kv, lane * 16 + 1024, s2, 16); zv1 = __builtin_amdgcn_raw_buffer_load_b128(rs_kv, lane * 16 + 1280, s2, 16);
          uv0 = __builtin_amdgcn_raw_buffer_load_b128(rs_kv, lane * 16 + 1024, s3, 16); uv1 = __builtin_amdgcn_raw_buffer_load_b128(rs_kv, lane * 16 + 1280, s3, 16); }
      zk = __builtin_amdgcn_raw_buffer_load_b128(rs_kv, lane * 16, s2, 16);
      uk = __builtin_amdgcn_raw_buffer_load_b128(rs_kv, lane * 16, s3, 16); }
    u64 fl = 0;
    if (w == 0) fl = __hip_atomic_load((GAS u64*)(kvf + t16 * 128), RLX_AGENT);
    float ps = 0.f;
    {
        const float xf = bflo(xs);
        float a[16];
#pragma unroll
        for (int j = 0; j < 4; ++j) { a[2 * j] = xf * bflo(wgv[j]); a[2 * j + 1] = xf * bfhi(wgv[j]); a[8 + 2 * j] = xf * bflo(wuv[j]); a[9 + 2 * j] = xf * bfhi(wuv[j]); }
#pragma unroll
        for (int j = 0; j < 8; ++j) { auto r = __builtin_amdgcn_permlane32_swap(__float_as_uint(a[j]), __float_as_uint(a[j + 8]), false, false); a[j] = __uint_as_float(r[0]) + __uint_as_float(r[1]); }
#pragma unroll
        for (int j = 0; j < 4; ++j) { auto r = __builtin_amdgcn_permlane16_swap(__float_as_uint(a[j]), __float_as_uint(a[j + 4]), false, false); a[j] = __uint_as_float(r[0]) + __uint_as_float(r[1]); }
        const f32x4 sm = {a[0] + dppf<0x128>(a[0]), a[1] + dppf<0x128>(a[1]), a[2] + dppf<0x128>(a[2]), a[3] + dppf<0x128>(a[3])};
        if ((lane & 8) == 0) *(LAS f32x4*)(smem + L_FS + w * 512 + (lane >> 5) * 256 + (lane & 7) * 32 + (g4 & 1) * 16) = sm;
        if (w == 0) km[0] = (unsigned)__ballot(fl == tag) & 0xffffu;
        lds_bar();
        if (w < 2) {
#pragma unroll
            for (int i = 0; i < 8; ++i) ps += *(const LAS float*)(smem + L_FS + i * 512 + tid * 4); }
    }
    STAMP(8);
    asm volatile("" :: "v"(yk), "v"(yv0), "v"(yv1), "v"(zk), "v"(zv0), "v"(zv1), "v"(uk), "v"(uv0), "v"(uv1), "v"(fl) : "memory");
    if (lane == 0) __hip_atomic_store((GAS u64*)(ws + WS_G) + tokr, ((u64)gbits << 48) | (tag & ((1ull << 48) - 1)), RLX_AGENT);
    if (ffn && w < 2) __hip_atomic_store((GAS u64*)(ws + WS_P1) + fi * 128 + tid, ((u64)(unsigned)tag << 32) | __float_as_uint(ps), RLX_AGENT);
    const int tb3 = 2 * xcd + (jj & 1), cg3 = jj >> 1;
    STAMP(16);
    { int gi = 0, cnt = 4, sb, t, u;
      const u64 vm = 0x0000ffff0000ffffull;
      int rdy = __builtin_amdgcn_readfirstlane(km[0]), m0s;
      unsigned wl = lds0 + L_WO + w * 4096, wv = (64 * w + (lane >> 2)) * 1024 + (((lane & 3) ^ ((lane >> 3) & 2)) << 4);
      asm volatile(
          "s_mov_b32 %[m0s], m0\n\ts_setprio 1\n\t" QKM(SA, "%[yk]", "%[q0]") "s_nop 11\n\t" EX(192) EX(193)
          "1:\n\t"
          TILE("y", "z", GROUP LOADB("0", "x"))
          TILE("z", "u", WODMA LOADB("0x2000", "y"))
          TILE("u", "x", LOADB("0x4000", "z"))
          "s_sub_u32 %[cnt], %[cnt], 1\n\ts_cmp_eq_u32 %[cnt], 0\n\ts_cbranch_scc1 4f\n\t"
          TILE("x", "y", LOADB("0x6000", "u"))
          "s_branch 1b\n\t"
          "4:\n\ts_mov_b32 m0, %[m0s]\n\ts_setprio 0\n\ts_waitcnt vmcnt(0)\n\ts_nop 11"
          : [o0] "+v"(o0), [o1] "+v"(o1), [o2] "+v"(o2), [o3] "+v"(o3),
            [xk] "+v"(kf), [xv0] "+v"(xv0), [xv1] "+v"(xv1), [yk] "+v"(yk), [yv0] "+v"(yv0), [yv1] "+v"(yv1),
            [zk] "+v"(zk), [zv0] "+v"(zv0), [zv1] "+v"(zv1), [uk] "+v"(uk), [uv0] "+v"(uv0), [uv1] "+v"(uv1),
            [gi] "+s"(gi), [cnt] "+s"(cnt), [rdy] "+s"(rdy), [spin] "+s"(spin),
            [sb] "=&s"(sb), [t] "=&s"(t), [u] "=&s"(u), [fl] "+v"(fl), [m0s] "=&s"(m0s), [wl] "+s"(wl), [wv] "+v"(wv)
          : [q0] "v"(q0), [q1] "v"(q1), [q2] "v"(q2), [q3] "v"(q3), [idx] "v"(idx), [ko] "v"(lane * 16),
            [rs] "s"(rs_kv), [vm] "s"(vm), [fo] "v"(t16 * 128), [fb] "s"(kvf), [tag] "s"(tag), [so] "s"(tb * 8192),
            [wp] "s"((const char*)p.wo + cg3 * 64)
          : S_CLOB, "vcc", "scc", "memory"); }
    STAMP(17);
    const bool own = (sel >> 7) == tb3;
    const __amdgpu_buffer_rsrc_t rs_y = __builtin_amdgcn_make_buffer_rsrc(ws + WS_YP + (size_t)w * 28 * 4096, 0, 28 * 4096, 0x00020000);
    u32x4 yp[7];
    auto loady = [&]() {
#pragma unroll
        for (int i = 0; i < 7; ++i) yp[i] = __builtin_amdgcn_raw_buffer_load_b128(rs_y, g4 * 4096 + cg3 * 256 + t16 * 16, i * 16384, 16); };
    const int tok = 128 * tb3 + 16 * w + t16;
    const GAS u64* gp = (const GAS u64*)(ws + WS_G) + tok;
    u64 gg = __hip_atomic_load(gp, RLX_AGENT);
    const u32x2 xa = *(const u32x2*)(p.x + (size_t)tok * 512 + 32 * cg3 + 4 * g4), xc = *(const u32x2*)(p.x + (size_t)tok * 512 + 32 * cg3 + 16 + 4 * g4);
    { auto put = [&](const f32x16& o, int j) {
          LAS char* d = smem + L_CB + ((hh * 4 + ((kq + j) & 3)) * 3 + (j - 1)) * 2304;
          *(LAS f32x4*)(d + lane * 16) = (f32x4){o[0], o[1], o[2], o[3]};
          *(LAS f32x4*)(d + 1024 + lane * 16) = (f32x4){o[4], o[5], o[6], o[7]};
          *(LAS float*)(d + 2048 + lane * 4) = o[8]; };
      put(o1, 1); put(o2, 2); put(o3, 3); }
    {
        unsigned tk = 0;
        if (lane == 0) tk = __hip_atomic_fetch_add((LAS unsigned*)(smem + L_KM + 8), 1u, __ATOMIC_RELAXED, __HIP_MEMORY_SCOPE_WORKGROUP);
        tk = __builtin_amdgcn_readfirstlane(tk);
        if (ffn && tk < 4) {
            const __amdgpu_buffer_rsrc_t rs_p = __builtin_amdgcn_make_buffer_rsrc(ws + WS_P1 + (size_t)(fi >> 3) * 8192, 0, 8192, 0x00020000);
            const int po = (lane >> 3) * 1024 + ((lane >> 2) & 1) * 512 + (fi & 7) * 64 + (lane & 3) * 16;
            u32x4 pv = __builtin_amdgcn_raw_buffer_load_b128(rs_p, po, 0, 16);
            unsigned wd2[8];
#pragma unroll
            for (int f = 0; f < 8; ++f) wd2[f] = *(const unsigned*)(p.wd + (size_t)(8 * fi + f) * 512 + 128 * tk + 2 * lane);
            while (!__all(pv[1] == (unsigned)tag && pv[3] == (unsigned)tag) && spin > 0) {
                --spin; asm volatile("" ::: "memory"); pv = __builtin_amdgcn_raw_buffer_load_b128(rs_p, po, 0, 16); }
            float hv[2];
#pragma unroll
            for (int j = 0; j < 2; ++j) {
                float v = __uint_as_float(pv[2 * j]); v += dppf<0x128>(v);
                auto a = __builtin_amdgcn_permlane16_swap(__float_as_uint(v), __float_as_uint(v), false, false); v = __uint_as_float(a[0]) + __uint_as_float(a[1]);
                auto b = __builtin_amdgcn_permlane32_swap(__float_as_uint(v), __float_as_uint(v), false, false);
                const float gs = bfr(__uint_as_float(b[0]) + __uint_as_float(b[1])), us = dppf<0x124>(gs);
                hv[j] = bfr(bfr(gs * sigmoidf_(gs)) * us); }
            float y0 = 0.f, y1 = 0.f;
#pragma unroll
            for (int f = 0; f < 8; ++f) {
                const float h = __int_as_float(__builtin_amdgcn_readlane(__float_as_int(hv[f & 1]), f >> 1));
                y0 = fmaf(h, bflo(wd2[f]), y0); y1 = fmaf(h, bfhi(wd2[f]), y1); }
            const __amdgpu_buffer_rsrc_t rs_w = __builtin_amdgcn_make_buffer_rsrc(ws + WS_YP + (size_t)fi * 4096, 0, 4096, 0x00020000);
            __builtin_amdgcn_raw_buffer_store_b128((u32x4){__float_as_uint(y0), (unsigned)tag, __float_as_uint(y1), (unsigned)tag}, rs_w, lane * 16, tk * 1024, 16);
        } }
    lds_bar();
    if (own) loady();
    STAMP(18);
    float o[9];
#pragma unroll
    for (int r = 0; r < 9; ++r) o[r] = o0[r];
#pragma unroll
    for (int j = 0; j < 3; ++j) {
        const LAS char* d = smem + L_CB + (w * 3 + j) * 2304;
        const f32x4 a = *(const LAS f32x4*)(d + lane * 16), b = *(const LAS f32x4*)(d + 1024 + lane * 16);
#pragma unroll
        for (int r = 0; r < 4; ++r) { o[r] += a[r]; o[4 + r] += b[r]; }
        o[8] += *(const LAS float*)(d + 2048 + lane * 4);
    }
    { float mag = 0.f;
#pragma unroll
      for (int r = 0; r < 8; ++r) mag += fabsf(o[r]);
      const bool safe = o[8] >= 9.313225746154785e-10f && o[8] <= 1.8446744073709552e19f && mag <= 3.0e38f;
      if (!__all(safe)) {
          f32x16 acc = {}; float m = -INFINITY;
          u32x4 v0 = {pat, pat, pat, pat}, v1 = v0;
          const __amdgpu_buffer_rsrc_t rs_all = __builtin_amdgcn_make_buffer_rsrc(ws + WS_KV + (size_t)head * 131072, 0, 131072, 0x00020000);
          for (int t = 0; t < 64; ++t) {
              const u32x4 kk = __builtin_amdgcn_raw_buffer_load_b128(rs_all, lane * 16, t * 2048, 16);
              if ((lane & 16) == 0) { v0 = __builtin_amdgcn_raw_buffer_load_b128(rs_all, lane * 16 + 1024, t * 2048, 16); v1 = __builtin_amdgcn_raw_buffer_load_b128(rs_all, lane * 16 + 1280, t * 2048, 16); }
              f32x16 s = __builtin_amdgcn_mfma_f32_32x32x16_bf16(__builtin_bit_cast(bf16x8, kk), __builtin_bit_cast(bf16x8, q0), (f32x16){}, 0, 0, 0);
              float tm = s[0];
#pragma unroll
              for (int r = 1; r < 16; ++r) tm = fmaxf(tm, s[r]);
              auto sw = __builtin_amdgcn_permlane32_swap(__float_as_uint(tm), __float_as_uint(tm), false, false);
              const float mn = fmaxf(m, fmaxf(__uint_as_float(sw[0]), __uint_as_float(sw[1])));
              const float al = __builtin_amdgcn_exp2f(m - mn); m = mn;
#pragma unroll
              for (int r = 0; r < 16; ++r) { s[r] = __builtin_amdgcn_exp2f(s[r] - mn); acc[r] *= al; }
              acc = __builtin_amdgcn_smfmac_f32_32x32x32_bf16(__builtin_bit_cast(bf16x8, v0), __builtin_bit_cast(bf16x16, __builtin_shufflevector(s, s, 0, 1, 2, 3, 4, 5, 6, 7)), acc, idx, 0, 0);
              acc = __builtin_amdgcn_smfmac_f32_32x32x32_bf16(__builtin_bit_cast(bf16x8, v1), __builtin_bit_cast(bf16x16, __builtin_shufflevector(s, s, 8, 9, 10, 11, 12, 13, 14, 15)), acc, idx, 0, 0);
          }
#pragma unroll
          for (int r = 0; r < 9; ++r) o[r] = acc[r];
      } }
    { const float inv = __builtin_amdgcn_rcpf(o[8]);
      const u32x4 of = {cvtpk(o[0] * inv, o[1] * inv), cvtpk(o[2] * inv, o[3] * inv), cvtpk(o[4] * inv, o[5] * inv), cvtpk(o[6] * inv, o[7] * inv)};
      __builtin_amdgcn_raw_buffer_store_b128(of, rs_os, lane * 16, 0, 16); }
    __builtin_amdgcn_s_waitcnt(0x0f70);
    asm volatile("s_waitcnt vmcnt(0) lgkmcnt(0)\n\ts_barrier" ::: "memory");
    if (tid == 0) __hip_atomic_store((GAS u64*)(ws + WS_OF + (tb * 16 + hp) * 128), tag, RLX_AGENT);
    STAMP(19); CSTAMP(30);

    if (own) {
        auto fresh = [&]() { bool ok = true;
#pragma unroll
            for (int i = 0; i < 7; ++i) ok = ok & (yp[i][1] == (unsigned)tag) & (yp[i][3] == (unsigned)tag);
            return __all(ok); };
        if (!fresh()) do { --spin; asm volatile("" ::: "memory"); loady(); } while (!fresh() && spin > 0);
        float s0 = 0.f, s1 = 0.f;
#pragma unroll
        for (int i = 0; i < 7; ++i) { s0 += __uint_as_float(yp[i][0]); s1 += __uint_as_float(yp[i][2]); }
        auto a = __builtin_amdgcn_permlane16_swap(__float_as_uint(s0), __float_as_uint(s1), false, false);
        const float v = __uint_as_float(a[0]) + __uint_as_float(a[1]);
        auto b = __builtin_amdgcn_permlane32_swap(__float_as_uint(v), __float_as_uint(v), false, false);
        if (lane < 32) *(LAS float*)(smem + L_YS + w * 128 + t16 * 8 + g4 * 4) = __uint_as_float(b[0]) + __uint_as_float(b[1]);
        lds_bar();
    }
    while (__any((gg ^ tag) << 16 != 0) && spin > 0) { --spin; gg = __hip_atomic_load(gp, RLX_AGENT); }
    const float gt = bfhi((unsigned)(gg >> 32));
    float bb[8] = {bflo(xa[0]), bfhi(xa[0]), bflo(xa[1]), bfhi(xa[1]), bflo(xc[0]), bfhi(xc[0]), bflo(xc[1]), bfhi(xc[1])};
    if (own && tok == sel) {
        f32x4 y0 = {}, y1 = {};
#pragma unroll
        for (int i = 0; i < 8; ++i) { y0 += *(const LAS f32x4*)(smem + L_YS + i * 128 + g4 * 16); y1 += *(const LAS f32x4*)(smem + L_YS + i * 128 + 64 + g4 * 16); }
#pragma unroll
        for (int r = 0; r < 4; ++r) { bb[r] = bfr(bb[r] + bfr(bfr(y0[r]) * rsel)); bb[4 + r] = bfr(bb[4 + r] + bfr(bfr(y1[r]) * rsel)); }
    }
    const float gm = bfr(1.0f - gt);
#pragma unroll
    for (int r = 0; r < 8; ++r) { bb[r] = bfr(gm * bb[r]); asm volatile("" : "+v"(bb[r])); }
    const __amdgpu_buffer_rsrc_t rs_o = __builtin_amdgcn_make_buffer_rsrc(ws + WS_O + (size_t)(4 * tb3 + (w >> 1)) * 32768, 0, 32768, 0x00020000);
    const int oo = (g4 >> 1) * 1024 + ((g4 & 1) * 32 + 16 * (w & 1) + t16) * 16;
    u32x4 ob[16];
    const LAS char* wa = smem + L_WO + ((g4 >> 1) * 16 + (g4 & 1) * 4 + ((lane & 15) >> 2)) * 64 + (lane & 3) * 8;
    f32x4 cg[4][2] = {};
    auto loadg = [&](int g) {
#pragma unroll
        for (int i = 4 * g; i < 4 * g + 4; ++i) { ob[i] = __builtin_amdgcn_raw_buffer_load_b128(rs_o, oo, i * 2048, 16); __builtin_amdgcn_sched_barrier(0); } };
    auto mulg = [&](int g) {
        const int h0 = (g4 & 1) << 5, h1 = h0 ^ 32; u32x4 wf[8];
#pragma unroll
        for (int i = 0; i < 4; ++i) { wf[2 * i] = trd2(wa + (4 * g + i) * 2048 + h0, 512); wf[2 * i + 1] = trd2(wa + (4 * g + i) * 2048 + h1, 512); }
        __builtin_amdgcn_sched_barrier(0);
#pragma unroll
        for (int i = 0; i < 4; ++i) { cg[g][0] = mfma16(wf[2 * i], ob[4 * g + i], cg[g][0]); cg[g][1] = mfma16(wf[2 * i + 1], ob[4 * g + i], cg[g][1]); }
        __builtin_amdgcn_sched_barrier(0); };
    const GAS u64* ofp = (const GAS u64*)(ws + WS_OF + (tb3 * 16 + t16) * 128);
    unsigned todo = 15;
    do {
        unsigned have;
        if (w == 0) { have = (unsigned)__ballot(__hip_atomic_load(ofp, RLX_AGENT) == tag); km[1] = have; }
        else { have = __builtin_amdgcn_readfirstlane(km[1]); __builtin_amdgcn_s_sleep(1); }
        if (spin <= 0) have = ~0u;
        --spin; asm volatile("" ::: "memory");
        if (todo == 15 && have == ~0u) {
            loadg(0); loadg(1); loadg(2); loadg(3); mulg(0); mulg(1); mulg(2); mulg(3);
            break; }
        unsigned go = 0;
#pragma unroll
        for (int g = 0; g < 4; ++g) if ((todo >> g & 1) && ((have >> (4 * g)) & 15u) == 15u) { go |= 1u << g; loadg(g); }
#pragma unroll
        for (int g = 0; g < 4; ++g) if (go >> g & 1) mulg(g);
        todo &= ~go;
    } while (todo);
    STAMP(20); CSTAMP(31);
    STAMP(21);
    const f32x4 c0 = (cg[0][0] + cg[1][0]) + (cg[2][0] + cg[3][0]), c1 = (cg[0][1] + cg[1][1]) + (cg[2][1] + cg[3][1]);
    f32x4 r0, r1;
#pragma unroll
    for (int r = 0; r < 4; ++r) { r0[r] = bfr(gt * bfr(c0[r])) + bb[r]; r1[r] = bfr(gt * bfr(c1[r])) + bb[4 + r]; }
    *(u32x4*)(p.out + (size_t)tok * 512 + 32 * cg3 + 16 * (g4 & 1) + 8 * (g4 >> 1)) = pack_pair(r0, r1);
}

extern "C" void kernel_launch(void* const* d_in, const int* in_sizes, int n_in,
                              void* d_out, int out_size, void* d_ws, size_t ws_size,
                              hipStream_t stream) {
    Params p;
    p.x = (const uint16_t*)d_in[0]; p.wqkv = (const uint16_t*)d_in[1]; p.wo = (const uint16_t*)d_in[2];
    p.wr = (const uint16_t*)d_in[4]; p.wg = (const uint16_t*)d_in[5]; p.wu = (const uint16_t*)d_in[6];
    p.wd = (const uint16_t*)d_in[7]; p.gw = (const uint16_t*)d_in[8];
    p.out = (uint16_t*)d_out; p.ws = (char*)d_ws;
    mix_kernel<<<256, 512, 0, stream>>>(p);
}
```

```cpp
#include <hip/hip_runtime.h>
#include <hip/hip_bf16.h>
#include <stdint.h>

typedef __attribute__((ext_vector_type(8))) __bf16 bf16x8;
typedef __attribute__((ext_vector_type(16))) __bf16 bf16x16;
typedef __attribute__((ext_vector_type(4))) float f32x4;
typedef __attribute__((ext_vector_type(8))) float f32x8;
typedef __attribute__((ext_vector_type(16))) float f32x16;
typedef __attribute__((ext_vector_type(4))) unsigned u32x4;
typedef __attribute__((ext_vector_type(2))) unsigned u32x2;
typedef __attribute__((ext_vector_type(4))) short s16x4;
typedef unsigned long long u64;
#define LAS __attribute__((address_space(3)))
#define GAS __attribute__((address_space(1)))
#define RLX_AGENT __ATOMIC_RELAXED, __HIP_MEMORY_SCOPE_AGENT

constexpr size_t WS_KV = 0;
constexpr size_t WS_O = 4u << 20;
constexpr size_t WS_YP = 6u << 20;
constexpr size_t WS_G = 7u << 20;
constexpr size_t WS_RG = WS_G + 16384;
constexpr size_t WS_KVF = WS_RG + 2048;
constexpr size_t WS_OF = WS_KVF + 32768;
constexpr size_t WS_P1 = WS_OF + 32768;
constexpr size_t WS_EP = WS_P1 + 229376;

constexpr int SLOT = 28672, NSLOT = 5;
constexpr int L_MISC = SLOT * NSLOT;
constexpr int L_RK = L_MISC;
constexpr int L_KM = L_MISC + 32;
constexpr int L_FS = 2 * SLOT;
constexpr int L_YS = L_MISC + 64;
constexpr int L_Q = L_MISC + 2048;
constexpr int L_RW = L_Q + 8192;
constexpr int LDS_BYTES = L_RW + 2048;
constexpr int L_WO = 3 * SLOT;
constexpr int L_CB = 0;
constexpr int SPIN_CAP = 200000;
#define WIN 3
#define RPOS 2
#define CODE_LINES 190
#define ROT 4
#define DSTEP 3
#define DOFF 0

#define STAMP(i)
#define CSTAMP(i)
#define STAMP_DECL
#define STAMP_OUT

struct Params {
    const uint16_t* x; const uint16_t* wqkv; const uint16_t* wo; const uint16_t* wr;
    const uint16_t* wg; const uint16_t* wu; const uint16_t* wd; const uint16_t* gw;
    uint16_t* out; char* ws;
};

__device__ __forceinline__ float bflo(unsigned u) { return __uint_as_float(u << 16); }
__device__ __forceinline__ float bfhi(unsigned u) { return __uint_as_float(u & 0xffff0000u); }
__device__ __forceinline__ unsigned cvtpk(float lo, float hi) {
    typedef __attribute__((ext_vector_type(2))) float f2; typedef __attribute__((ext_vector_type(2))) __bf16 b2;
    return __builtin_bit_cast(unsigned, __builtin_convertvector((f2){lo, hi}, b2));
}
__device__ __forceinline__ float bfr(float v) { return bflo(cvtpk(v, 0.f) & 0xffffu); }
__device__ __forceinline__ float sigmoidf_(float v) { return __builtin_amdgcn_rcpf(1.0f + __builtin_amdgcn_exp2f(v * -1.4426950408889634f)); }
__device__ __forceinline__ f32x4 mfma16(u32x4 a, u32x4 b, f32x4 c) {
    return __builtin_amdgcn_mfma_f32_16x16x32_bf16(__builtin_bit_cast(bf16x8, a), __builtin_bit_cast(bf16x8, b), c, 0, 0, 0);
}
__device__ __forceinline__ u32x2 trd(const LAS char* p) {
    return __builtin_bit_cast(u32x2, __builtin_amdgcn_ds_read_tr16_b64_v4i16((LAS s16x4*)p));
}
__device__ __forceinline__ u32x4 trd2(const LAS char* p, int d) { u32x2 a = trd(p), b = trd(p + d); return (u32x4){a[0], a[1], b[0], b[1]}; }
__device__ __forceinline__ void dma16(const void* sbase, unsigned voff, unsigned lds) {
    unsigned keep;
    asm volatile("s_mov_b32 %0, m0\n\ts_mov_b32 m0, %3\n\ts_nop 0\n\tglobal_load_lds_dwordx4 %1, %2\n\ts_mov_b32 m0, %0"
                 : "=&s"(keep) : "v"(voff), "s"(sbase), "s"(lds) : "memory");
}
__device__ __forceinline__ void dma16_half(const void* sbase, unsigned voff, unsigned lds) {
    unsigned keep;
    asm volatile("s_mov_b32 %0, m0\n\ts_mov_b32 m0, %3\n\ts_mov_b32 exec_hi, 0\n\tglobal_load_lds_dwordx4 %1, %2\n\ts_mov_b32 exec_hi, -1\n\ts_mov_b32 m0, %0"
                 : "=&s"(keep) : "v"(voff), "s"(sbase), "s"(lds) : "memory");
}
template <int N> __device__ __forceinline__ void wait_bar() {
    asm volatile("" ::: "memory"); __builtin_amdgcn_s_waitcnt(0xc07f);
    asm volatile("s_waitcnt vmcnt(%0) lgkmcnt(0)\n\ts_barrier" :: "n"(N) : "memory"); }
template <int N> __device__ __forceinline__ void vm_bar() { asm volatile("s_waitcnt vmcnt(%0)\n\ts_barrier" :: "n"(N) : "memory"); }
__device__ __forceinline__ void lds_bar() { asm volatile("s_waitcnt lgkmcnt(0)\n\ts_barrier" ::: "memory"); }
__device__ __forceinline__ void bare_bar() { asm volatile("s_barrier" ::: "memory"); }
template <int CTRL> __device__ __forceinline__ float dppf(float v) {
    return __int_as_float(__builtin_amdgcn_update_dpp(0, __float_as_int(v), CTRL, 0xf, 0xf, true));
}
template <int CTRL> __device__ __forceinline__ unsigned dppu(unsigned v) {
    return (unsigned)__builtin_amdgcn_update_dpp(0, (int)v, CTRL, 0xf, 0xf, true);
}
__device__ __forceinline__ float row_sum16(float v) {
    v += dppf<0x128>(v); v += dppf<0x124>(v); v += dppf<0x122>(v); v += dppf<0x121>(v); return v;
}
__device__ __forceinline__ float wave_sum(float v) {
    v = row_sum16(v);
    auto a = __builtin_amdgcn_permlane16_swap(__float_as_uint(v), __float_as_uint(v), false, false);
    v = __uint_as_float(a[0]) + __uint_as_float(a[1]);
    auto b = __builtin_amdgcn_permlane32_swap(__float_as_uint(v), __float_as_uint(v), false, false);
    return __uint_as_float(b[0]) + __uint_as_float(b[1]);
}
__device__ __forceinline__ unsigned wave_maxu(unsigned v) {
    v = max(v, dppu<0x128>(v)); v = max(v, dppu<0x124>(v)); v = max(v, dppu<0x122>(v)); v = max(v, dppu<0x121>(v));
    auto a = __builtin_amdgcn_permlane16_swap(v, v, false, false); v = max(a[0], a[1]);
    auto b = __builtin_amdgcn_permlane32_swap(v, v, false, false); return max(b[0], b[1]);
}
__device__ __forceinline__ u32x4 pack_pair(f32x4 a, f32x4 b) {
    auto x = __builtin_amdgcn_permlane16_swap(cvtpk(a[0], a[1]), cvtpk(b[0], b[1]), false, false);
    auto y = __builtin_amdgcn_permlane16_swap(cvtpk(a[2], a[3]), cvtpk(b[2], b[3]), false, false);
    return (u32x4){x[0], y[0], x[1], y[1]};
}
#define STR2(x) #x
#define STR(x) STR2(x)
#define EX(r) "v_exp_f32 v" STR(r) ", v" STR(r) "\n\t"
#define QKM(S, K, Q) "v_mfma_f32_32x32x16_bf16 " S ", " K ", " Q ", 0\n\t"
#define PVM(O, V, P) "v_smfmac_f32_32x32x32_bf16 " O ", " V ", " P ", %[idx]\n\t"
#define UNIT_A(O, V0, V1, MID) EX(194) EX(195) EX(196) MID EX(197) EX(198) EX(199) EX(200) EX(201) PVM(O, V0, "v[192:199]") \
    EX(202) EX(203) EX(204) EX(205) EX(206) EX(207) EX(208) EX(209) PVM(O, V1, "v[200:207]")
#define UNIT_B(O, V0, V1, MID) EX(210) EX(211) EX(212) MID EX(213) EX(214) EX(215) EX(216) EX(217) PVM(O, V0, "v[208:215]") \
    EX(218) EX(219) EX(220) EX(221) EX(222) EX(223) EX(192) EX(193) PVM(O, V1, "v[216:223]")
#define UNIT_B_LAST(O, V0, V1) EX(210) EX(211) EX(212) EX(213) EX(214) EX(215) EX(216) EX(217) PVM(O, V0, "v[208:215]") \
    EX(218) EX(219) EX(220) EX(221) EX(222) EX(223) "s_nop 1\n\t" PVM(O, V1, "v[216:223]")
#define SA "v[192:207]"
#define SB "v[208:223]"
#define S_CLOB "v192", "v193", "v194", "v195", "v196", "v197", "v198", "v199", "v200", "v201", "v202", "v203", "v204", "v205", "v206", "v207", \
    "v208", "v209", "v210", "v211", "v212", "v213", "v214", "v215", "v216", "v217", "v218", "v219", "v220", "v221", "v222", "v223"
#define LOADT(J, K, V0, V1) "s_xor_b32 %[t], %[sb], " J "\n\t" \
    "buffer_load_dwordx4 " K ", %[ko], %[rs], %[t] offen sc1\n\t" \
    "s_mov_b64 exec, %[vm]\n\t" \
    "buffer_load_dwordx4 " V0 ", %[ko], %[rs], %[t] offen offset:1024 sc1\n\t" \
    "buffer_load_dwordx4 " V1 ", %[ko], %[rs], %[t] offen offset:1280 sc1\n\t" \
    "s_mov_b64 exec, -1\n\t"
#define GROUP "s_add_u32 %[gi], %[gi], 0x8000\n\ts_and_b32 %[gi], %[gi], 0x18000\n\ts_xor_b32 %[sb], %[so], %[gi]\n\t" \
    "s_lshr_b32 %[t], %[sb], 13\n\ts_and_b32 %[t], %[t], 12\n\ts_lshl_b32 %[t], 15, %[t]\n\t" \
    "s_andn2_b32 %[u], %[t], %[rdy]\n\ts_cbranch_scc0 3f\n\t" \
    "2:\n\ts_waitcnt vmcnt(0)\n\t" \
    "v_cmp_eq_u64 vcc, %[tag], %[fl]\n\ts_nop 4\n\ts_and_b32 %[rdy], vcc_lo, 0xffff\n\t" \
    "s_andn2_b32 %[u], %[t], %[rdy]\n\ts_cbranch_scc0 3f\n\t" \
    "global_load_dwordx2 %[fl], %[fo], %[fb] sc1\n\t" \
    "s_sub_i32 %[spin], %[spin], 1\n\ts_cmp_gt_i32 %[spin], 0\n\ts_cbranch_scc1 2b\n\t" \
    "3:\n\t"
#define TILE(B, BN, LD) \
    UNIT_A("%[o0]", "%[" B "v0]", "%[" B "v1]", QKM(SB, "%[" B "k]", "%[q1]") LD) \
    UNIT_B("%[o1]", "%[" B "v0]", "%[" B "v1]", QKM(SA, "%[" B "k]", "%[q2]")) \
    UNIT_A("%[o2]", "%[" B "v0]", "%[" B "v1]", QKM(SB, "%[" B "k]", "%[q3]")) \
    UNIT_B("%[o3]", "%[" B "v0]", "%[" B "v1]", "s_waitcnt vmcnt(6)\n\t" QKM(SA, "%[" BN "k]", "%[q0]"))
#define WODMA "s_mov_b32 m0, %[wl]\n\ts_add_u32 %[wl], %[wl], 0x400\n\tglobal_load_lds_dwordx4 %[wv], %[wp]\n\tv_add_u32 %[wv], 0x4000, %[wv]\n\t"
#define LOADB(J, B) LOADT(J, "%[" B "k]", "%[" B "v0]", "%[" B "v1]")

__global__ __launch_bounds__(512, 2) void mix_kernel(Params p) {
    __shared__ __attribute__((aligned(16))) char smem_[LDS_BYTES];
    LAS char* smem = (LAS char*)smem_;
    const unsigned lds0 = (unsigned)(uintptr_t)smem;
    const int tid = threadIdx.x, lane = tid & 63;
    const int w = __builtin_amdgcn_readfirstlane(tid >> 6);
    const int xcd = blockIdx.x & 7, jj = blockIdx.x >> 3;
    const int tb = 4 * (xcd >> 1) + (jj & 3), hp = 8 * (xcd & 1) + (jj >> 2);
    const int kq = w & 3, hh = w >> 2, head = 2 * hp + hh;
    const int g4 = lane >> 4, t16 = lane & 15;
    char* const ws = p.ws;
    const __amdgpu_buffer_rsrc_t rs_kv = __builtin_amdgcn_make_buffer_rsrc(ws + WS_KV + (size_t)head * 131072 + kq * 2048, 0, 131072 - kq * 2048, 0x00020000);
    int spin = SPIN_CAP;
    STAMP_DECL; STAMP(0);
    asm volatile("" :: "s"(p.x), "s"(p.wqkv), "s"(p.wo), "s"(p.wr), "s"(p.wg), "s"(p.wu), "s"(p.wd), "s"(p.gw), "s"(p.out), "s"(p.ws));
    STAMP(24);

    const int tokr = 128 * tb + 8 * hp + w;
    u64 ep;
    asm volatile("s_load_dwordx2 %0, %1, 0x0" : "=s"(ep) : "s"(ws + WS_EP) : "memory");
    if (w < 2) dma16(w ? p.gw : p.wr, lane * 16, lds0 + L_RW + w * 1024);
    u32x4 rx;
    asm volatile("global_load_dwordx4 %0, %1, %2" : "=v"(rx) : "v"(lane * 16), "s"(p.x + (size_t)tokr * 512) : "memory");
    unsigned xo0, xo1, wo0, wo1;
    { const int t0 = 16 * w + (lane >> 3), t1 = t0 + 8;
      xo0 = t0 * 1024 + (((lane & 7) ^ ((t0 >> 1) & 7)) << 4);
      xo1 = t1 * 1024 + (((lane & 7) ^ ((t1 >> 1) & 7)) << 4);
      const int k0 = 16 * (w & 3) + (lane >> 2), pc = ((lane & 3) ^ (((k0 >> 3) & 1) << 1)) << 4;
      wo0 = k0 * 3072 + (w >> 2) * 1024 + pc;
      const int k1 = 8 * w + ((lane >> 2) & 7);
      wo1 = k1 * 3072 + 2048 + (((lane & 3) ^ ((w & 1) << 1)) << 4); }
    const char* xb = (const char*)p.x + (size_t)tb * 131072;
    const char* wb = (const char*)p.wqkv + hp * 64;
    const int rot = ROT == 0 ? 0 : ROT == 1 ? (2 * (jj & 3) + (jj >> 2)) & 7 : ROT == 2 ? (((jj & 3) + (jj >> 2)) & 1) * 4 : ((jj & 3) + (jj >> 2) * 2 + (jj >> 3)) & 3;
    auto issue = [&](int c, int i) {
        const unsigned s = lds0 + (c % NSLOT) * SLOT; const int cc = ROT == 4 ? c ^ rot : (c + rot) & 7;
        if (i == 0) dma16(xb + cc * 128, xo0, s + w * 2048);
        if (i == 1) dma16(wb + cc * 196608, wo0, s + 16384 + w * 1024);
        if (i == 2) dma16(xb + cc * 128, xo1, s + w * 2048 + 1024);
        if (i == 3) dma16_half(wb + cc * 196608, wo1, s + 16384 + 8192 + w * 512);
    };
    const __amdgpu_buffer_rsrc_t rs_os = __builtin_amdgcn_make_buffer_rsrc(ws + WS_O + (size_t)(4 * tb + kq) * 32768 + head * 1024, 0, 1024, 0x00020000);
#pragma unroll
    for (int i = 0; i < 4; ++i) issue(0, i);
    if (tid == 0) *(LAS u32x4*)(smem + L_KM) = (u32x4){0u, 0u, 0u, 0u};
    asm volatile("s_waitcnt vmcnt(4) lgkmcnt(0)\n\ts_barrier" : "+v"(rx), "+s"(ep) :: "memory");
    const u64 tag = ep + 1;
    unsigned gbits, pf;
    {
            const u32x4 rw = *(const LAS u32x4*)(smem + L_RW + lane * 16), rgw = *(const LAS u32x4*)(smem + L_RW + 1024 + lane * 16);
            float ra = 0.f, ga = 0.f;
#pragma unroll
            for (int j = 0; j < 4; ++j) {
                const float a = bflo(rx[j]), b = bfhi(rx[j]);
                ra = fmaf(a, bflo(rw[j]), ra); ra = fmaf(b, bfhi(rw[j]), ra);
                const unsigned pr = cvtpk(a * bflo(rgw[j]), b * bfhi(rgw[j]));
                ga += bflo(pr); ga += bfhi(pr); }
            ra = wave_sum(ra); ga = wave_sum(ga);
            const unsigned rb = cvtpk(ra, 0.f) & 0xffffu;
            const unsigned key = (((rb & 0x8000u) ? (~rb & 0xffffu) : (rb | 0x8000u)) << 11) | (unsigned)(2047 - tokr);
            gbits = cvtpk(sigmoidf_(bfr(ga)), 0.f) & 0xffffu;
            if (lane == 0) *(LAS unsigned*)(smem + L_RK + 4 * w) = key; }
#pragma unroll
    for (int c = 1; c < WIN; ++c) {
#pragma unroll
        for (int i = 0; i < 4; ++i) issue(c, i);
        if (c == 1) { lds_bar();
            if (w == 0) {
                const unsigned kk = wave_maxu(*(const LAS unsigned*)(smem + L_RK + 4 * (lane & 7)));
                if (lane == 0) __hip_atomic_store((GAS u64*)(ws + WS_RG) + blockIdx.x, ((u64)kk << 37) | (tag & ((1ull << 37) - 1)), RLX_AGENT);
            }
        } else if (c + 1 < WIN) bare_bar();
    }
    const int fi = min((int)blockIdx.x, 223); const bool ffn = blockIdx.x < 224;
    const int frow = 64 * (fi & 7) + 8 * w + (lane >> 3);
    u32x4 wgv, wuv;
    const __amdgpu_buffer_rsrc_t rs_rg = __builtin_amdgcn_make_buffer_rsrc(ws + WS_RG, 0, 2048, 0x00020000);
    STAMP(1);
    u32x4 kf, vc, qf;
    {   f32x4 aq[2] = {}, ak[2] = {}, av[2] = {};
        const int fsw = (t16 >> 1) & 7;
        const int xa = (32 * kq + t16) * 128 + ((g4 ^ fsw) << 4);
        const int wa = 16384 + g4 * 512 + ((lane & 15) >> 2) * 64 + ((hh ^ (g4 & 1)) << 5) + (lane & 3) * 8;
        u32x4 fx[2][4], fw[2][6];
        auto frag = [&](int c, int i) {
            const LAS char* s = smem + (c % NSLOT) * SLOT; const int kk = i / 5, j = i % 5;
            if (j == 1 || j == 2) fx[c & 1][2 * kk + j - 1] = *(const LAS u32x4*)(s + (xa ^ (kk * 64)) + (j - 1) * 2048);
            else { const int m = j ? j - 2 : 0; fw[c & 1][3 * kk + m] = trd2(s + wa + m * 4096 + kk * 2048, 256); } };
        wait_bar<4 * (WIN - 1)>();
        STAMP(3); CSTAMP(24);
#pragma unroll
        for (int i = 0; i < 10; ++i) frag(0, i);
#pragma unroll
        for (int c = 0; c < 8; ++c) {
            if (c < 7) {
                const int last = c + WIN - 1 < 7 ? c + WIN - 1 : 7;
                switch (4 * (last - c - 1) + (c > 7 - WIN ? 3 : 0)) { case 4: vm_bar<4>(); break; case 8: vm_bar<8>(); break; case 7: vm_bar<7>(); break;
                    case 11: vm_bar<11>(); break; default: vm_bar<3>(); } }
            if (c == 1) { STAMP(6); CSTAMP(25); }
            if (c == 2) { STAMP(11); CSTAMP(26); }
            if (c == 3) { STAMP(7); CSTAMP(27); }
            if (c == 5) { STAMP(9); CSTAMP(28); }
            if (c == 6) { STAMP(10); }
            __builtin_amdgcn_sched_barrier(0);
#pragma unroll
            for (int i = 0; i < 12; ++i) {
                const int kk = i / 6, j = i % 6; const u32x4 xx = fx[c & 1][2 * kk + (j & 1)], ww = fw[c & 1][3 * kk + (j >> 1)];
                if (j < 2) aq[j] = mfma16(ww, xx, aq[j]); else if (j < 4) ak[j - 2] = mfma16(ww, xx, ak[j - 2]); else av[j - 4] = mfma16(xx, ww, av[j - 4]);
                if (c < 7 && i < 5) { frag(c + 1, 2 * i); frag(c + 1, 2 * i + 1); }
                if (c + WIN < 8 && i % DSTEP == DOFF && i / DSTEP < 4) issue(c + WIN, i / DSTEP);
                __builtin_amdgcn_sched_barrier(0);
            }
            if (c == 7 - WIN) {
                wgv = *(const u32x4*)(p.wg + (size_t)frow * 1792 + 64 * (fi >> 3) + 8 * (lane & 7));
                wuv = *(const u32x4*)(p.wu + (size_t)frow * 1792 + 64 * (fi >> 3) + 8 * (lane & 7));
                asm volatile("global_load_dword %0, %1, %2" : "=v"(pf) : "v"((jj * 128 + w * 16 + t16) * 128), "s"(p.wo) : "memory"); }
        }
        constexpr float QS = 0.25f * 1.4426950408889634f;
        kf = pack_pair(ak[0], ak[1]); vc = pack_pair(av[0], av[1]);
        __builtin_amdgcn_raw_buffer_store_b128(kf, rs_kv, lane * 16, tb * 8192, 16);
        __builtin_amdgcn_raw_buffer_store_b128(vc, rs_kv, lane * 16 + 1024, tb * 8192, 16);
        __builtin_amdgcn_sched_barrier(0);
        qf = pack_pair(aq[0] * QS, aq[1] * QS);
        *(LAS u32x4*)(smem + L_Q + w * 1024 + lane * 16) = qf;
        __builtin_amdgcn_sched_barrier(0);
    }
    STAMP(12); CSTAMP(29);

    const unsigned pat = ((lane & 11) == 0) ? 0x3f803f80u : 0u;
    u32x4 xv0, xv1;
#pragma unroll
    for (int j = 0; j < 4; ++j) { auto r = __builtin_amdgcn_permlane16_swap(vc[j], pat, false, false); xv0[j] = r[0]; xv1[j] = r[1]; }
    f32x16 o0 = {}, o1 = {}, o2 = {}, o3 = {};
    const int idx = 0xdddd;
    const char* kvf = ws + WS_KVF + hp * 2048;
    lds_bar();
    const u32x4 q0 = qf;
    const u32x4 q1 = *(const LAS u32x4*)(smem + L_Q + (hh * 4 + ((kq + 1) & 3)) * 1024 + lane * 16);
    const u32x4 q2 = *(const LAS u32x4*)(smem + L_Q + (hh * 4 + ((kq + 2) & 3)) * 1024 + lane * 16);
    const u32x4 q3 = *(const LAS u32x4*)(smem + L_Q + (hh * 4 + ((kq + 3) & 3)) * 1024 + lane * 16);
    auto own_pair = [&](f32x16& oa, f32x16& ob_, const u32x4& qa, const u32x4& qb) {
        asm volatile(
            "s_nop 1\n\t" QKM(SA, "%[k]", "%[q0]") "s_nop 11\n\t" EX(192) EX(193)
            UNIT_A("%[o0]", "%[v0]", "%[v1]", QKM(SB, "%[k]", "%[q1]"))
            UNIT_B_LAST("%[o1]", "%[v0]", "%[v1]") "s_nop 11"
            : [o0] "+v"(oa), [o1] "+v"(ob_)
            : [k] "v"(kf), [v0] "v"(xv0), [v1] "v"(xv1), [q0] "v"(qa), [q1] "v"(qb), [idx] "v"(idx)
            : S_CLOB); };
    own_pair(o0, o1, q0, q1);
    STAMP(13);
    asm volatile("s_waitcnt vmcnt(0)\n\ts_barrier" :: "v"(pf) : "memory");
    if (tid == 0) __hip_atomic_store((GAS u64*)(kvf + tb * 128), tag, RLX_AGENT);
    u32x4 rg0 = __builtin_amdgcn_raw_buffer_load_b128(rs_rg, lane * 32, 0, 16), rg1 = __builtin_amdgcn_raw_buffer_load_b128(rs_rg, lane * 32 + 16, 0, 16);
    __builtin_amdgcn_sched_barrier(0);
    own_pair(o2, o3, q2, q3);
    asm volatile("" : "+v"(rg0), "+v"(rg1));
    STAMP(15);
    { const unsigned tl = (unsigned)tag, th = (unsigned)(tag >> 32) & 31u;
      auto fresh = [&]() { return __all((((rg0[0] ^ tl) | (rg0[2] ^ tl) | (rg1[0] ^ tl) | (rg1[2] ^ tl)) | (((rg0[1] ^ th) | (rg0[3] ^ th) | (rg1[1] ^ th) | (rg1[3] ^ th)) & 31u)) == 0); };
      if (!fresh()) do {
          --spin; asm volatile("" ::: "memory");
          rg0 = __builtin_amdgcn_raw_buffer_load_b128(rs_rg, lane * 32, 0, 16); rg1 = __builtin_amdgcn_raw_buffer_load_b128(rs_rg, lane * 32 + 16, 0, 16);
      } while (!fresh() && spin > 0); }
    int sel; float rsel;
    { const unsigned k0 = max(max(rg0[1], rg0[3]), max(rg1[1], rg1[3])) >> 5;
      const unsigned kk = __builtin_amdgcn_readfirstlane(wave_maxu(k0));
      sel = 2047 - (int)(kk & 2047u);
      const unsigned mono = kk >> 11, rb = (mono & 0x8000u) ? (mono & 0x7fffu) : (~mono & 0xffffu);
      rsel = bfr(sigmoidf_(bflo(rb))); }
    const unsigned xs = p.x[(size_t)sel * 512 + frow];
    if (blockIdx.x == 0 && tid == 0) __hip_atomic_store((GAS u64*)(ws + WS_EP), tag, RLX_AGENT);
    STAMP(14);

    volatile LAS unsigned* km = (volatile LAS unsigned*)(smem + L_KM);
    { const unsigned need = (15u << (tb & 12)) & ~(1u << tb);
      for (;;) {
          unsigned m;
          if (w == 0) { m = (unsigned)__ballot(__hip_atomic_load((GAS u64*)(kvf + t16 * 128), RLX_AGENT) == tag) & 0xffffu; km[0] = m; }
          else { m = __builtin_amdgcn_readfirstlane(km[0]); __builtin_amdgcn_s_sleep(1); }
          if ((m & need) == need || spin <= 0) break;
          --spin; }
      asm volatile("" :: "v"(xs), "v"(wgv), "v"(wuv) : "memory"); }
    STAMP(2);
    u32x4 yk, yv0, yv1, zk, zv0, zv1, uk, uv0, uv1;
    yv0 = yv1 = zv0 = zv1 = uv0 = uv1 = (u32x4){pat, pat, pat, pat};
    { const int s1 = (tb ^ 1) * 8192, s2 = (tb ^ 2) * 8192, s3 = (tb ^ 3) * 8192;
      yk = __builtin_amdgcn_raw_buffer_load_b128(rs_kv, lane * 16, s1, 16);
      if ((lane & 16) == 0) {
          yv0 = __builtin_amdgcn_raw_buffer_load_b128(rs_kv, lane * 16 + 1024, s1, 16); yv1 = __builtin_amdgcn_raw_buffer_load_b128(rs_kv, lane * 16 + 1280, s1, 16);
          zv0 = __builtin_amdgcn_raw_buffer_load_b128(rs_kv, lane * 16 + 1024, s2, 16); zv1 = __builtin_amdgcn_raw_buffer_load_b128(rs_kv, lane * 16 + 1280, s2, 16);
          uv0 = __builtin_amdgcn_raw_buffer_load_b128(rs_kv, lane * 16 + 1024, s3, 16); uv1 = __builtin_amdgcn_raw_buffer_load_b128(rs_kv, lane * 16 + 1280, s3, 16); }
      zk = __builtin_amdgcn_raw_buffer_load_b128(rs_kv, lane * 16, s2, 16);
      uk = __builtin_amdgcn_raw_buffer_load_b128(rs_kv, lane * 16, s3, 16); }
    u64 fl = 0;
    if (w == 0) fl = __hip_atomic_load((GAS u64*)(kvf + t16 * 128), RLX_AGENT);
    float ps = 0.f;
    {
        const float xf = bflo(xs);
        float a[16];
#pragma unroll
        for (int j = 0; j < 4; ++j) { a[2 * j] = xf * bflo(wgv[j]); a[2 * j + 1] = xf * bfhi(wgv[j]); a[8 + 2 * j] = xf * bflo(wuv[j]); a[9 + 2 * j] = xf * bfhi(wuv[j]); }
#pragma unroll
        for (int j = 0; j < 8; ++j) { auto r = __builtin_amdgcn_permlane32_swap(__float_as_uint(a[j]), __float_as_uint(a[j + 8]), false, false); a[j] = __uint_as_float(r[0]) + __uint_as_float(r[1]); }
#pragma unroll
        for (int j = 0; j < 4; ++j) { auto r = __builtin_amdgcn_permlane16_swap(__float_as_uint(a[j]), __float_as_uint(a[j + 4]), false, false); a[j] = __uint_as_float(r[0]) + __uint_as_float(r[1]); }
        const f32x4 sm = {a[0] + dppf<0x128>(a[0]), a[1] + dppf<0x128>(a[1]), a[2] + dppf<0x128>(a[2]), a[3] + dppf<0x128>(a[3])};
        if ((lane & 8) == 0) *(LAS f32x4*)(smem + L_FS + w * 512 + (lane >> 5) * 256 + (lane & 7) * 32 + (g4 & 1) * 16) = sm;
        if (w == 0) km[0] = (unsigned)__ballot(fl == tag) & 0xffffu;
        lds_bar();
        if (w < 2) {
#pragma unroll
            for (int i = 0; i < 8; ++i) ps += *(const LAS float*)(smem + L_FS + i * 512 + tid * 4); }
    }
    STAMP(8);
    asm volatile("" :: "v"(yk), "v"(yv0), "v"(yv1), "v"(zk), "v"(zv0), "v"(zv1), "v"(uk), "v"(uv0), "v"(uv1), "v"(fl) : "memory");
    if (lane == 0) __hip_atomic_store((GAS u64*)(ws + WS_G) + tokr, ((u64)gbits << 48) | (tag & ((1ull << 48) - 1)), RLX_AGENT);
    if (ffn && w < 2) __hip_atomic_store((GAS u64*)(ws + WS_P1) + fi * 128 + tid, ((u64)(unsigned)tag << 32) | __float_as_uint(ps), RLX_AGENT);
    const int tb3 = 2 * xcd + (jj & 1), cg3 = jj >> 1;
    STAMP(16);
    { int gi = 0, cnt = 4, sb, t, u;
      const u64 vm = 0x0000ffff0000ffffull;
      int rdy = __builtin_amdgcn_readfirstlane(km[0]), m0s;
      unsigned wl = lds0 + L_WO + w * 4096, wv = (64 * w + (lane >> 2)) * 1024 + (((lane & 3) ^ ((lane >> 3) & 2)) << 4);
      asm volatile(
          "s_mov_b32 %[m0s], m0\n\ts_setprio 1\n\t" QKM(SA, "%[yk]", "%[q0]") "s_nop 11\n\t" EX(192) EX(193)
          "1:\n\t"
          TILE("y", "z", GROUP LOADB("0", "x"))
          TILE("z", "u", WODMA LOADB("0x2000", "y"))
          TILE("u", "x", LOADB("0x4000", "z"))
          "s_sub_u32 %[cnt], %[cnt], 1\n\ts_cmp_eq_u32 %[cnt], 0\n\ts_cbranch_scc1 4f\n\t"
          TILE("x", "y", LOADB("0x6000", "u"))
          "s_branch 1b\n\t"
          "4:\n\ts_mov_b32 m0, %[m0s]\n\ts_setprio 0\n\ts_waitcnt vmcnt(0)\n\ts_nop 11"
          : [o0] "+v"(o0), [o1] "+v"(o1), [o2] "+v"(o2), [o3] "+v"(o3),
            [xk] "+v"(kf), [xv0] "+v"(xv0), [xv1] "+v"(xv1), [yk] "+v"(yk), [yv0] "+v"(yv0), [yv1] "+v"(yv1),
            [zk] "+v"(zk), [zv0] "+v"(zv0), [zv1] "+v"(zv1), [uk] "+v"(uk), [uv0] "+v"(uv0), [uv1] "+v"(uv1),
            [gi] "+s"(gi), [cnt] "+s"(cnt), [rdy] "+s"(rdy), [spin] "+s"(spin),
            [sb] "=&s"(sb), [t] "=&s"(t), [u] "=&s"(u), [fl] "+v"(fl), [m0s] "=&s"(m0s), [wl] "+s"(wl), [wv] "+v"(wv)
          : [q0] "v"(q0), [q1] "v"(q1), [q2] "v"(q2), [q3] "v"(q3), [idx] "v"(idx), [ko] "v"(lane * 16),
            [rs] "s"(rs_kv), [vm] "s"(vm), [fo] "v"(t16 * 128), [fb] "s"(kvf), [tag] "s"(tag), [so] "s"(tb * 8192),
            [wp] "s"((const char*)p.wo + cg3 * 64)
          : S_CLOB, "vcc", "scc", "memory"); }
    STAMP(17);
    const bool own = (sel >> 7) == tb3;
    const __amdgpu_buffer_rsrc_t rs_y = __builtin_amdgcn_make_buffer_rsrc(ws + WS_YP + (size_t)w * 28 * 4096, 0, 28 * 4096, 0x00020000);
    u32x4 yp[7];
    auto loady = [&]() {
#pragma unroll
        for (int i = 0; i < 7; ++i) yp[i] = __builtin_amdgcn_raw_buffer_load_b128(rs_y, g4 * 4096 + cg3 * 256 + t16 * 16, i * 16384, 16); };
    const int tok = 128 * tb3 + 16 * w + t16;
    const GAS u64* gp = (const GAS u64*)(ws + WS_G) + tok;
    u64 gg = __hip_atomic_load(gp, RLX_AGENT);
    const u32x2 xa = *(const u32x2*)(p.x + (size_t)tok * 512 + 32 * cg3 + 4 * g4), xc = *(const u32x2*)(p.x + (size_t)tok * 512 + 32 * cg3 + 16 + 4 * g4);
    { auto put = [&](const f32x16& o, int j) {
          LAS char* d = smem + L_CB + ((hh * 4 + ((kq + j) & 3)) * 3 + (j - 1)) * 2304;
          *(LAS f32x4*)(d + lane * 16) = (f32x4){o[0], o[1], o[2], o[3]};
          *(LAS f32x4*)(d + 1024 + lane * 16) = (f32x4){o[4], o[5], o[6], o[7]};
          *(LAS float*)(d + 2048 + lane * 4) = o[8]; };
      put(o1, 1); put(o2, 2); put(o3, 3); }
    {
        unsigned tk = 0;
        if (lane == 0) tk = __hip_atomic_fetch_add((LAS unsigned*)(smem + L_KM + 8), 1u, __ATOMIC_RELAXED, __HIP_MEMORY_SCOPE_WORKGROUP);
        tk = __builtin_amdgcn_readfirstlane(tk);
        if (ffn && tk < 4) {
            const __amdgpu_buffer_rsrc_t rs_p = __builtin_amdgcn_make_buffer_rsrc(ws + WS_P1 + (size_t)(fi >> 3) * 8192, 0, 8192, 0x00020000);
            const int po = (lane >> 3) * 1024 + ((lane >> 2) & 1) * 512 + (fi & 7) * 64 + (lane & 3) * 16;
            u32x4 pv = __builtin_amdgcn_raw_buffer_load_b128(rs_p, po, 0, 16);
            unsigned wd2[8];
#pragma unroll
            for (int f = 0; f < 8; ++f) wd2[f] = *(const unsigned*)(p.wd + (size_t)(8 * fi + f) * 512 + 128 * tk + 2 * lane);
            while (!__all(pv[1] == (unsigned)tag && pv[3] == (unsigned)tag) && spin > 0) {
                --spin; asm volatile("" ::: "memory"); pv = __builtin_amdgcn_raw_buffer_load_b128(rs_p, po, 0, 16); }
            float hv[2];
#pragma unroll
            for (int j = 0; j < 2; ++j) {
                float v = __uint_as_float(pv[2 * j]); v += dppf<0x128>(v);
                auto a = __builtin_amdgcn_permlane16_swap(__float_as_uint(v), __float_as_uint(v), false, false); v = __uint_as_float(a[0]) + __uint_as_float(a[1]);
                auto b = __builtin_amdgcn_permlane32_swap(__float_as_uint(v), __float_as_uint(v), false, false);
                const float gs = bfr(__uint_as_float(b[0]) + __uint_as_float(b[1])), us = dppf<0x124>(gs);
                hv[j] = bfr(bfr(gs * sigmoidf_(gs)) * us); }
            float y0 = 0.f, y1 = 0.f;
#pragma unroll
            for (int f = 0; f < 8; ++f) {
                const float h = __int_as_float(__builtin_amdgcn_readlane(__float_as_int(hv[f & 1]), f >> 1));
                y0 = fmaf(h, bflo(wd2[f]), y0); y1 = fmaf(h, bfhi(wd2[f]), y1); }
            const __amdgpu_buffer_rsrc_t rs_w = __builtin_amdgcn_make_buffer_rsrc(ws + WS_YP + (size_t)fi * 4096, 0, 4096, 0x00020000);
            __builtin_amdgcn_raw_buffer_store_b128((u32x4){__float_as_uint(y0), (unsigned)tag, __float_as_uint(y1), (unsigned)tag}, rs_w, lane * 16, tk * 1024, 16);
        } }
    lds_bar();
    if (own) loady();
    STAMP(18);
    float o[9];
#pragma unroll
    for (int r = 0; r < 9; ++r) o[r] = o0[r];
#pragma unroll
    for (int j = 0; j < 3; ++j) {
        const LAS char* d = smem + L_CB + (w * 3 + j) * 2304;
        const f32x4 a = *(const LAS f32x4*)(d + lane * 16), b = *(const LAS f32x4*)(d + 1024 + lane * 16);
#pragma unroll
        for (int r = 0; r < 4; ++r) { o[r] += a[r]; o[4 + r] += b[r]; }
        o[8] += *(const LAS float*)(d + 2048 + lane * 4);
    }
    { float mag = 0.f;
#pragma unroll
      for (int r = 0; r < 8; ++r) mag += fabsf(o[r]);
      const bool safe = o[8] >= 9.313225746154785e-10f && o[8] <= 1.8446744073709552e19f && mag <= 3.0e38f;
      if (!__all(safe)) {
          f32x16 acc = {}; float m = -INFINITY;
          u32x4 v0 = {pat, pat, pat, pat}, v1 = v0;
          const __amdgpu_buffer_rsrc_t rs_all = __builtin_amdgcn_make_buffer_rsrc(ws + WS_KV + (size_t)head * 131072, 0, 131072, 0x00020000);
          for (int t = 0; t < 64; ++t) {
              const u32x4 kk = __builtin_amdgcn_raw_buffer_load_b128(rs_all, lane * 16, t * 2048, 16);
              if ((lane & 16) == 0) { v0 = __builtin_amdgcn_raw_buffer_load_b128(rs_all, lane * 16 + 1024, t * 2048, 16); v1 = __builtin_amdgcn_raw_buffer_load_b128(rs_all, lane * 16 + 1280, t * 2048, 16); }
              f32x16 s = __builtin_amdgcn_mfma_f32_32x32x16_bf16(__builtin_bit_cast(bf16x8, kk), __builtin_bit_cast(bf16x8, q0), (f32x16){}, 0, 0, 0);
              float tm = s[0];
#pragma unroll
              for (int r = 1; r < 16; ++r) tm = fmaxf(tm, s[r]);
              auto sw = __builtin_amdgcn_permlane32_swap(__float_as_uint(tm), __float_as_uint(tm), false, false);
              const float mn = fmaxf(m, fmaxf(__uint_as_float(sw[0]), __uint_as_float(sw[1])));
              const float al = __builtin_amdgcn_exp2f(m - mn); m = mn;
#pragma unroll
              for (int r = 0; r < 16; ++r) { s[r] = __builtin_amdgcn_exp2f(s[r] - mn); acc[r] *= al; }
              acc = __builtin_amdgcn_smfmac_f32_32x32x32_bf16(__builtin_bit_cast(bf16x8, v0), __builtin_bit_cast(bf16x16, __builtin_shufflevector(s, s, 0, 1, 2, 3, 4, 5, 6, 7)), acc, idx, 0, 0);
              acc = __builtin_amdgcn_smfmac_f32_32x32x32_bf16(__builtin_bit_cast(bf16x8, v1), __builtin_bit_cast(bf16x16, __builtin_shufflevector(s, s, 8, 9, 10, 11, 12, 13, 14, 15)), acc, idx, 0, 0);
          }
#pragma unroll
          for (int r = 0; r < 9; ++r) o[r] = acc[r];
      } }
    { const float inv = __builtin_amdgcn_rcpf(o[8]);
      const u32x4 of = {cvtpk(o[0] * inv, o[1] * inv), cvtpk(o[2] * inv, o[3] * inv), cvtpk(o[4] * inv, o[5] * inv), cvtpk(o[6] * inv, o[7] * inv)};
      __builtin_amdgcn_raw_buffer_store_b128(of, rs_os, lane * 16, 0, 16); }
    __builtin_amdgcn_s_waitcnt(0x0f70);
    asm volatile("s_waitcnt vmcnt(0) lgkmcnt(0)\n\ts_barrier" ::: "memory");
    if (tid == 0) __hip_atomic_store((GAS u64*)(ws + WS_OF + (tb * 16 + hp) * 128), tag, RLX_AGENT);
    STAMP(19); CSTAMP(30);

    if (own) {
        auto fresh = [&]() { bool ok = true;
#pragma unroll
            for (int i = 0; i < 7; ++i) ok = ok & (yp[i][1] == (unsigned)tag) & (yp[i][3] == (unsigned)tag);
            return __all(ok); };
        if (!fresh()) do { --spin; asm volatile("" ::: "memory"); loady(); } while (!fresh() && spin > 0);
        float s0 = 0.f, s1 = 0.f;
#pragma unroll
        for (int i = 0; i < 7; ++i) { s0 += __uint_as_float(yp[i][0]); s1 += __uint_as_float(yp[i][2]); }
        auto a = __builtin_amdgcn_permlane16_swap(__float_as_uint(s0), __float_as_uint(s1), false, false);
        const float v = __uint_as_float(a[0]) + __uint_as_float(a[1]);
        auto b = __builtin_amdgcn_permlane32_swap(__float_as_uint(v), __float_as_uint(v), false, false);
        if (lane < 32) *(LAS float*)(smem + L_YS + w * 128 + t16 * 8 + g4 * 4) = __uint_as_float(b[0]) + __uint_as_float(b[1]);
        lds_bar();
    }
    while (__any((gg ^ tag) << 16 != 0) && spin > 0) { --spin; gg = __hip_atomic_load(gp, RLX_AGENT); }
    const float gt = bfhi((unsigned)(gg >> 32));
    float bb[8] = {bflo(xa[0]), bfhi(xa[0]), bflo(xa[1]), bfhi(xa[1]), bflo(xc[0]), bfhi(xc[0]), bflo(xc[1]), bfhi(xc[1])};
    if (own && tok == sel) {
        f32x4 y0 = {}, y1 = {};
#pragma unroll
        for (int i = 0; i < 8; ++i) { y0 += *(const LAS f32x4*)(smem + L_YS + i * 128 + g4 * 16); y1 += *(const LAS f32x4*)(smem + L_YS + i * 128 + 64 + g4 * 16); }
#pragma unroll
        for (int r = 0; r < 4; ++r) { bb[r] = bfr(bb[r] + bfr(bfr(y0[r]) * rsel)); bb[4 + r] = bfr(bb[4 + r] + bfr(bfr(y1[r]) * rsel)); }
    }
    const float gm = bfr(1.0f - gt);
#pragma unroll
    for (int r = 0; r < 8; ++r) { bb[r] = bfr(gm * bb[r]); asm volatile("" : "+v"(bb[r])); }
    const __amdgpu_buffer_rsrc_t rs_o = __builtin_amdgcn_make_buffer_rsrc(ws + WS_O + (size_t)(4 * tb3 + (w >> 1)) * 32768, 0, 32768, 0x00020000);
    const int oo = (g4 >> 1) * 1024 + ((g4 & 1) * 32 + 16 * (w & 1) + t16) * 16;
    u32x4 ob[16];
    const LAS char* wa = smem + L_WO + ((g4 >> 1) * 16 + (g4 & 1) * 4 + ((lane & 15) >> 2)) * 64 + (lane & 3) * 8;
    f32x4 cg[4][2] = {};
    auto loadg = [&](int g) {
#pragma unroll
        for (int i = 4 * g; i < 4 * g + 4; ++i) { ob[i] = __builtin_amdgcn_raw_buffer_load_b128(rs_o, oo, i * 2048, 16); __builtin_amdgcn_sched_barrier(0); } };
    auto mulg = [&](int g) {
        const int h0 = (g4 & 1) << 5, h1 = h0 ^ 32; u32x4 wf[8];
#pragma unroll
        for (int i = 0; i < 4; ++i) { wf[2 * i] = trd2(wa + (4 * g + i) * 2048 + h0, 512); wf[2 * i + 1] = trd2(wa + (4 * g + i) * 2048 + h1, 512); }
        __builtin_amdgcn_sched_barrier(0);
#pragma unroll
        for (int i = 0; i < 4; ++i) { cg[g][0] = mfma16(wf[2 * i], ob[4 * g + i], cg[g][0]); cg[g][1] = mfma16(wf[2 * i + 1], ob[4 * g + i], cg[g][1]); }
        __builtin_amdgcn_sched_barrier(0); };
    const GAS u64* ofp = (const GAS u64*)(ws + WS_OF + (tb3 * 16 + t16) * 128);
    unsigned todo = 15;
    do {
        unsigned have;
        if (w == 0) { have = (unsigned)__ballot(__hip_atomic_load(ofp, RLX_AGENT) == tag); km[1] = have; }
        else { have = __builtin_amdgcn_readfirstlane(km[1]); __builtin_amdgcn_s_sleep(1); }
        if (spin <= 0) have = ~0u;
        --spin; asm volatile("" ::: "memory");
        if (todo == 15 && have == ~0u) {
            loadg(0); loadg(1); loadg(2); loadg(3); mulg(0); mulg(1); mulg(2); mulg(3);
            break; }
        unsigned go = 0;
#pragma unroll
        for (int g = 0; g < 4; ++g) if ((todo >> g & 1) && ((have >> (4 * g)) & 15u) == 15u) { go |= 1u << g; loadg(g); }
#pragma unroll
        for (int g = 0; g < 4; ++g) if (go >> g & 1) mulg(g);
        todo &= ~go;
    } while (todo);
    STAMP(20); CSTAMP(31);
    STAMP(21);
    const f32x4 c0 = (cg[0][0] + cg[1][0]) + (cg[2][0] + cg[3][0]), c1 = (cg[0][1] + cg[1][1]) + (cg[2][1] + cg[3][1]);
    f32x4 r0, r1;
#pragma unroll
    for (int r = 0; r < 4; ++r) { r0[r] = bfr(gt * bfr(c0[r])) + bb[r]; r1[r] = bfr(gt * bfr(c1[r])) + bb[4 + r]; }
#define OAUX 0
    { const __amdgpu_buffer_rsrc_t rs_out = __builtin_amdgcn_make_buffer_rsrc(p.out, 0, 2u << 20, 0x00020000);
      __builtin_amdgcn_raw_buffer_store_b128(pack_pair(r0, r1), rs_out, tok * 1024 + 64 * cg3 + 32 * (g4 & 1) + 16 * (g4 >> 1), 0, OAUX); }
}

extern "C" void kernel_launch(void* const* d_in, const int* in_sizes, int n_in,
                              void* d_out, int out_size, void* d_ws, size_t ws_size,
                              hipStream_t stream) {
    Params p;
    p.x = (const uint16_t*)d_in[0]; p.wqkv = (const uint16_t*)d_in[1]; p.wo = (const uint16_t*)d_in[2];
    p.wr = (const uint16_t*)d_in[4]; p.wg = (const uint16_t*)d_in[5]; p.wu = (const uint16_t*)d_in[6];
    p.wd = (const uint16_t*)d_in[7]; p.gw = (const uint16_t*)d_in[8];
    p.out = (uint16_t*)d_out; p.ws = (char*)d_ws;
    mix_kernel<<<256, 512, 0, stream>>>(p);

}
```

```cpp
#include <hip/hip_runtime.h>
#include <hip/hip_bf16.h>
#include <stdint.h>

typedef __attribute__((ext_vector_type(8))) __bf16 bf16x8;
typedef __attribute__((ext_vector_type(16))) __bf16 bf16x16;
typedef __attribute__((ext_vector_type(4))) float f32x4;
typedef __attribute__((ext_vector_type(8))) float f32x8;
typedef __attribute__((ext_vector_type(16))) float f32x16;
typedef __attribute__((ext_vector_type(4))) unsigned u32x4;
typedef __attribute__((ext_vector_type(2))) unsigned u32x2;
typedef __attribute__((ext_vector_type(4))) short s16x4;
typedef unsigned long long u64;
#define LAS __attribute__((address_space(3)))
#define GAS __attribute__((address_space(1)))
#define RLX_AGENT __ATOMIC_RELAXED, __HIP_MEMORY_SCOPE_AGENT

constexpr size_t WS_KV = 0;
constexpr size_t WS_O = 4u << 20;
constexpr size_t WS_YP = 6u << 20;
constexpr size_t WS_G = 7u << 20;
constexpr size_t WS_RG = WS_G + 16384;
constexpr size_t WS_KVF = WS_RG + 2048;
constexpr size_t WS_OF = WS_KVF + 32768;
constexpr size_t WS_P1 = WS_OF + 32768;
constexpr size_t WS_EP = WS_P1 + 229376;

constexpr int SLOT = 24576, NSLOT = 5;
constexpr int L_WQ = SLOT * NSLOT;
constexpr int L_MISC = L_WQ + 32768;
constexpr int L_RK = L_MISC;
constexpr int L_KM = L_MISC + 32;
constexpr int L_YS = L_MISC + 64;
constexpr int L_RW = L_MISC + 2048;
constexpr int LDS_BYTES = L_RW + 2048;
constexpr int L_Q = 0;
constexpr int L_CB = 0;
constexpr int L_FS = 3 * SLOT;
constexpr int L_WO = L_FS + 8192;
constexpr int SPIN_CAP = 200000;
#define POLL 0
#define PFQ 0
#define ROT 4

#define STAMP(i)
#define CSTAMP(i)
#define STAMP_DECL
#define STAMP_OUT

struct Params {
    const uint16_t* x; const uint16_t* wqkv; const uint16_t* wo; const uint16_t* wr;
    const uint16_t* wg; const uint16_t* wu; const uint16_t* wd; const uint16_t* gw;
    uint16_t* out; char* ws;
};

__device__ __forceinline__ float bflo(unsigned u) { return __uint_as_float(u << 16); }
__device__ __forceinline__ float bfhi(unsigned u) { return __uint_as_float(u & 0xffff0000u); }
__device__ __forceinline__ unsigned cvtpk(float lo, float hi) {
    typedef __attribute__((ext_vector_type(2))) float f2; typedef __attribute__((ext_vector_type(2))) __bf16 b2;
    return __builtin_bit_cast(unsigned, __builtin_convertvector((f2){lo, hi}, b2));
}
__device__ __forceinline__ float bfr(float v) { return bflo(cvtpk(v, 0.f) & 0xffffu); }
__device__ __forceinline__ float sigmoidf_(float v) { return __builtin_amdgcn_rcpf(1.0f + __builtin_amdgcn_exp2f(v * -1.4426950408889634f)); }
__device__ __forceinline__ f32x4 mfma16(u32x4 a, u32x4 b, f32x4 c) {
    return __builtin_amdgcn_mfma_f32_16x16x32_bf16(__builtin_bit_cast(bf16x8, a), __builtin_bit_cast(bf16x8, b), c, 0, 0, 0);
}
__device__ __forceinline__ u32x2 trd(const LAS char* p) {
    return __builtin_bit_cast(u32x2, __builtin_amdgcn_ds_read_tr16_b64_v4i16((LAS s16x4*)p));
}
__device__ __forceinline__ u32x4 trd2(const LAS char* p, int d) { u32x2 a = trd(p), b = trd(p + d); return (u32x4){a[0], a[1], b[0], b[1]}; }
__device__ __forceinline__ void dma16(const void* sbase, unsigned voff, unsigned lds) {
    unsigned keep;
    asm volatile("s_mov_b32 %0, m0\n\ts_mov_b32 m0, %3\n\ts_nop 0\n\tglobal_load_lds_dwordx4 %1, %2\n\ts_mov_b32 m0, %0"
                 : "=&s"(keep) : "v"(voff), "s"(sbase), "s"(lds) : "memory");
}
__device__ __forceinline__ void dma16_half(const void* sbase, unsigned voff, unsigned lds) {
    unsigned keep;
    asm volatile("s_mov_b32 %0, m0\n\ts_mov_b32 m0, %3\n\ts_mov_b32 exec_hi, 0\n\tglobal_load_lds_dwordx4 %1, %2\n\ts_mov_b32 exec_hi, -1\n\ts_mov_b32 m0, %0"
                 : "=&s"(keep) : "v"(voff), "s"(sbase), "s"(lds) : "memory");
}
template <int N> __device__ __forceinline__ void wait_bar() {
    asm volatile("" ::: "memory"); __builtin_amdgcn_s_waitcnt(0xc07f);
    asm volatile("s_waitcnt vmcnt(%0) lgkmcnt(0)\n\ts_barrier" :: "n"(N) : "memory"); }
template <int N> __device__ __forceinline__ void vm_bar() { asm volatile("s_waitcnt vmcnt(%0)\n\ts_barrier" :: "n"(N) : "memory"); }
__device__ __forceinline__ void lds_bar() { asm volatile("s_waitcnt lgkmcnt(0)\n\ts_barrier" ::: "memory"); }
__device__ __forceinline__ void bare_bar() { asm volatile("s_barrier" ::: "memory"); }
template <int CTRL> __device__ __forceinline__ float dppf(float v) {
    return __int_as_float(__builtin_amdgcn_update_dpp(0, __float_as_int(v), CTRL, 0xf, 0xf, true));
}
template <int CTRL> __device__ __forceinline__ unsigned dppu(unsigned v) {
    return (unsigned)__builtin_amdgcn_update_dpp(0, (int)v, CTRL, 0xf, 0xf, true);
}
__device__ __forceinline__ float row_sum16(float v) {
    v += dppf<0x128>(v); v += dppf<0x124>(v); v += dppf<0x122>(v); v += dppf<0x121>(v); return v;
}
__device__ __forceinline__ float wave_sum(float v) {
    v = row_sum16(v);
    auto a = __builtin_amdgcn_permlane16_swap(__float_as_uint(v), __float_as_uint(v), false, false);
    v = __uint_as_float(a[0]) + __uint_as_float(a[1]);
    auto b = __builtin_amdgcn_permlane32_swap(__float_as_uint(v), __float_as_uint(v), false, false);
    return __uint_as_float(b[0]) + __uint_as_float(b[1]);
}
__device__ __forceinline__ unsigned wave_maxu(unsigned v) {
    v = max(v, dppu<0x128>(v)); v = max(v, dppu<0x124>(v)); v = max(v, dppu<0x122>(v)); v = max(v, dppu<0x121>(v));
    auto a = __builtin_amdgcn_permlane16_swap(v, v, false, false); v = max(a[0], a[1]);
    auto b = __builtin_amdgcn_permlane32_swap(v, v, false, false); return max(b[0], b[1]);
}
__device__ __forceinline__ u32x4 pack_pair(f32x4 a, f32x4 b) {
    auto x = __builtin_amdgcn_permlane16_swap(cvtpk(a[0], a[1]), cvtpk(b[0], b[1]), false, false);
    auto y = __builtin_amdgcn_permlane16_swap(cvtpk(a[2], a[3]), cvtpk(b[2], b[3]), false, false);
    return (u32x4){x[0], y[0], x[1], y[1]};
}
#define STR2(x) #x
#define STR(x) STR2(x)
#define EX(r) "v_exp_f32 v" STR(r) ", v" STR(r) "\n\t"
#define QKM(S, K, Q) "v_mfma_f32_32x32x16_bf16 " S ", " K ", " Q ", 0\n\t"
#define PVM(O, V, P) "v_smfmac_f32_32x32x32_bf16 " O ", " V ", " P ", %[idx]\n\t"
#define UNIT_A(O, V0, V1, MID) EX(194) EX(195) EX(196) MID EX(197) EX(198) EX(199) EX(200) EX(201) PVM(O, V0, "v[192:199]") \
    EX(202) EX(203) EX(204) EX(205) EX(206) EX(207) EX(208) EX(209) PVM(O, V1, "v[200:207]")
#define UNIT_B(O, V0, V1, MID) EX(210) EX(211) EX(212) MID EX(213) EX(214) EX(215) EX(216) EX(217) PVM(O, V0, "v[208:215]") \
    EX(218) EX(219) EX(220) EX(221) EX(222) EX(223) EX(192) EX(193) PVM(O, V1, "v[216:223]")
#define UNIT_B_LAST(O, V0, V1) EX(210) EX(211) EX(212) EX(213) EX(214) EX(215) EX(216) EX(217) PVM(O, V0, "v[208:215]") \
    EX(218) EX(219) EX(220) EX(221) EX(222) EX(223) "s_nop 1\n\t" PVM(O, V1, "v[216:223]")
#define SA "v[192:207]"
#define SB "v[208:223]"
#define S_CLOB "v192", "v193", "v194", "v195", "v196", "v197", "v198", "v199", "v200", "v201", "v202", "v203", "v204", "v205", "v206", "v207", \
    "v208", "v209", "v210", "v211", "v212", "v213", "v214", "v215", "v216", "v217", "v218", "v219", "v220", "v221", "v222", "v223"
#define LOADT(J, K, V0, V1) "s_xor_b32 %[t], %[sb], " J "\n\t" \
    "buffer_load_dwordx4 " K ", %[ko], %[rs], %[t] offen sc1\n\t" \
    "s_mov_b64 exec, %[vm]\n\t" \
    "buffer_load_dwordx4 " V0 ", %[ko], %[rs], %[t] offen offset:1024 sc1\n\t" \
    "buffer_load_dwordx4 " V1 ", %[ko], %[rs], %[t] offen offset:1280 sc1\n\t" \
    "s_mov_b64 exec, -1\n\t"
#define GROUP "s_add_u32 %[gi], %[gi], 0x8000\n\ts_and_b32 %[gi], %[gi], 0x18000\n\ts_xor_b32 %[sb], %[so], %[gi]\n\t" \
    "s_lshr_b32 %[t], %[sb], 13\n\ts_and_b32 %[t], %[t], 12\n\ts_lshl_b32 %[t], 15, %[t]\n\t" \
    "s_andn2_b32 %[u], %[t], %[rdy]\n\ts_cbranch_scc0 3f\n\t" \
    "2:\n\ts_waitcnt vmcnt(0)\n\t" \
    "v_cmp_eq_u64 vcc, %[tag], %[fl]\n\ts_nop 4\n\ts_and_b32 %[rdy], vcc_lo, 0xffff\n\t" \
    "s_andn2_b32 %[u], %[t], %[rdy]\n\ts_cbranch_scc0 3f\n\t" \
    "global_load_dwordx2 %[fl], %[fo], %[fb] sc1\n\t" \
    "s_sub_i32 %[spin], %[spin], 1\n\ts_cmp_gt_i32 %[spin], 0\n\ts_cbranch_scc1 2b\n\t" \
    "3:\n\t"
#define TILE(B, BN, LD) \
    UNIT_A("%[o0]", "%[" B "v0]", "%[" B "v1]", QKM(SB, "%[" B "k]", "%[q1]") LD) \
    UNIT_B("%[o1]", "%[" B "v0]", "%[" B "v1]", QKM(SA, "%[" B "k]", "%[q2]")) \
    UNIT_A("%[o2]", "%[" B "v0]", "%[" B "v1]", QKM(SB, "%[" B "k]", "%[q3]")) \
    UNIT_B("%[o3]", "%[" B "v0]", "%[" B "v1]", "s_waitcnt vmcnt(6)\n\t" QKM(SA, "%[" BN "k]", "%[q0]"))
#define WODMA "s_mov_b32 m0, %[wl]\n\ts_add_u32 %[wl], %[wl], 0x400\n\tglobal_load_lds_dwordx4 %[wv], %[wp]\n\tv_add_u32 %[wv], 0x4000, %[wv]\n\t"
#define LOADB(J, B) LOADT(J, "%[" B "k]", "%[" B "v0]", "%[" B "v1]")

__global__ __launch_bounds__(512, 2) void mix_kernel(Params p) {
    __shared__ __attribute__((aligned(16))) char smem_[LDS_BYTES];
    LAS char* smem = (LAS char*)smem_;
    const unsigned lds0 = (unsigned)(uintptr_t)smem;
    const int tid = threadIdx.x, lane = tid & 63;
    const int w = __builtin_amdgcn_readfirstlane(tid >> 6);
    const int xcd = blockIdx.x & 7, jj = blockIdx.x >> 3;
    const int tb = 4 * (xcd >> 1) + (jj & 3), hp = 8 * (xcd & 1) + (jj >> 2);
    const int kq = w & 3, hh = w >> 2, head = 2 * hp + hh;
    const int g4 = lane >> 4, t16 = lane & 15;
    char* const ws = p.ws;
    const __amdgpu_buffer_rsrc_t rs_kv = __builtin_amdgcn_make_buffer_rsrc(ws + WS_KV + (size_t)head * 131072 + kq * 2048, 0, 131072 - kq * 2048, 0x00020000);
    int spin = SPIN_CAP;
    STAMP_DECL; STAMP(0);
    asm volatile("" :: "s"(p.x), "s"(p.wqkv), "s"(p.wo), "s"(p.wr), "s"(p.wg), "s"(p.wu), "s"(p.wd), "s"(p.gw), "s"(p.out), "s"(p.ws));
    STAMP(24);

    const int tokr = 128 * tb + 8 * hp + w;
    u64 ep;
    asm volatile("s_load_dwordx2 %0, %1, 0x0" : "=s"(ep) : "s"(ws + WS_EP) : "memory");
    if (w < 2) dma16(w ? p.gw : p.wr, lane * 16, lds0 + L_RW + w * 1024);
    unsigned pq = 0;
    if (PFQ && w == 7) asm volatile("global_load_dword %0, %1, %2" : "=v"(pq) : "v"((jj * 16 + (lane >> 2)) * 3072 + (xcd & 1) * 512 + (lane & 3) * 128), "s"(p.wqkv) : "memory");
    u32x4 rx;
    asm volatile("global_load_dwordx4 %0, %1, %2" : "=v"(rx) : "v"(lane * 16), "s"(p.x + (size_t)tokr * 512) : "memory");
    unsigned xo0, xo1, wo0;
    { const int t0 = 16 * w + (lane >> 3), t1 = t0 + 8;
      xo0 = t0 * 1024 + (((lane & 7) ^ ((t0 >> 1) & 7)) << 4);
      xo1 = t1 * 1024 + (((lane & 7) ^ ((t1 >> 1) & 7)) << 4);
      const int k0 = 16 * (w & 3) + (lane >> 2), pc = ((lane & 3) ^ (((k0 >> 3) & 1) << 1)) << 4;
      wo0 = k0 * 3072 + 1024 + (w >> 2) * 1024 + pc; }
    const char* xb = (const char*)p.x + (size_t)tb * 131072;
    const char* wb = (const char*)p.wqkv + hp * 64;
    const int rot = ROT == 0 ? 0 : ROT == 1 ? (2 * (jj & 3) + (jj >> 2)) & 7 : ROT == 2 ? (((jj & 3) + (jj >> 2)) & 1) * 4 : ((jj & 3) + (jj >> 2) * 2 + (jj >> 3)) & 3;
    const int rx4 = ROT == 5 ? rot ^ (((xcd ^ (xcd >> 1)) & 1) << 2) : rot;
    auto chunk = [&](int c) { return ROT >= 4 ? c ^ rx4 : (c + rot) & 7; };
    auto issue = [&](int c, int i) {
        const unsigned s = lds0 + (c % NSLOT) * SLOT; const int cc = chunk(c);
        if (i == 0) dma16(xb + cc * 128, xo0, s + w * 2048);
        if (i == 1) dma16(wb + cc * 196608, wo0, s + 16384 + w * 1024);
        if (i == 2) dma16(xb + cc * 128, xo1, s + w * 2048 + 1024);
    };
    const __amdgpu_buffer_rsrc_t rs_os = __builtin_amdgcn_make_buffer_rsrc(ws + WS_O + (size_t)(4 * tb + kq) * 32768 + head * 1024, 0, 1024, 0x00020000);
#pragma unroll
    for (int i = 0; i < 3; ++i) issue(0, i);
    if (tid == 0) *(LAS u32x4*)(smem + L_KM) = (u32x4){0u, 0u, 0u, 0u};
    asm volatile("s_waitcnt vmcnt(3) lgkmcnt(0)\n\ts_barrier" : "+v"(rx), "+s"(ep) : "v"(pq) : "memory");
    const u64 tag = ep + 1;
    unsigned gbits;
    {
            const u32x4 rw = *(const LAS u32x4*)(smem + L_RW + lane * 16), rgw = *(const LAS u32x4*)(smem + L_RW + 1024 + lane * 16);
            float ra = 0.f, ga = 0.f;
#pragma unroll
            for (int j = 0; j < 4; ++j) {
                const float a = bflo(rx[j]), b = bfhi(rx[j]);
                ra = fmaf(a, bflo(rw[j]), ra); ra = fmaf(b, bfhi(rw[j]), ra);
                const unsigned pr = cvtpk(a * bflo(rgw[j]), b * bfhi(rgw[j]));
                ga += bflo(pr); ga += bfhi(pr); }
            ra = wave_sum(ra); ga = wave_sum(ga);
            const unsigned rb = cvtpk(ra, 0.f) & 0xffffu;
            const unsigned key = (((rb & 0x8000u) ? (~rb & 0xffffu) : (rb | 0x8000u)) << 11) | (unsigned)(2047 - tokr);
            gbits = cvtpk(sigmoidf_(bfr(ga)), 0.f) & 0xffffu;
            if (lane == 0) *(LAS unsigned*)(smem + L_RK + 4 * w) = key; }
#pragma unroll
    for (int c = 1; c < 3; ++c) {
#pragma unroll
        for (int i = 0; i < 3; ++i) issue(c, i);
        if (c == 1) { lds_bar();
            if (w == 0) {
                const unsigned kk = wave_maxu(*(const LAS unsigned*)(smem + L_RK + 4 * (lane & 7)));
                if (lane == 0) __hip_atomic_store((GAS u64*)(ws + WS_RG) + blockIdx.x, ((u64)kk << 37) | (tag & ((1ull << 37) - 1)), RLX_AGENT);
            }
        }
    }
    const int fi = min((int)blockIdx.x, 223); const bool ffn = blockIdx.x < 224;
    const int frow = 64 * (fi & 7) + 8 * w + (lane >> 3);
    const __amdgpu_buffer_rsrc_t rs_rg = __builtin_amdgcn_make_buffer_rsrc(ws + WS_RG, 0, 2048, 0x00020000);
    STAMP(1);
    u32x4 kf, vc, qf;
    {   f32x4 aq[2] = {}, ak[2] = {}, av[2] = {};
        const int fsw = (t16 >> 1) & 7;
        const int xa = (32 * kq + t16) * 128 + ((g4 ^ fsw) << 4);
        const int wa = g4 * 512 + ((lane & 15) >> 2) * 64 + ((hh ^ (g4 & 1)) << 5) + (lane & 3) * 8;
        u32x4 fx[8][4], fw[2][4];
        auto frag = [&](int c, int i) {
            const LAS char* s = smem + (c % NSLOT) * SLOT; const int kk = i >> 2, j = i & 3;
            if (j == 1 || j == 2) fx[c][2 * kk + j - 1] = *(const LAS u32x4*)(s + (xa ^ (kk * 64)) + (j - 1) * 2048);
            else fw[c & 1][2 * kk + (j ? 1 : 0)] = trd2(s + 16384 + wa + (j ? 4096 : 0) + kk * 2048, 256); };
        wait_bar<6>();
        STAMP(3); CSTAMP(24);
#pragma unroll
        for (int i = 0; i < 8; ++i) { frag(0, i); if (i < 3) issue(3, i); __builtin_amdgcn_sched_barrier(0); }
#pragma unroll
        for (int c = 0; c < 8; ++c) {
            if (c < 5) vm_bar<6>(); else if (c == 5) vm_bar<5>(); else if (c == 6) vm_bar<4>();
            if (c == 1) { STAMP(6); CSTAMP(25); }
            if (c == 2) { STAMP(11); CSTAMP(26); }
            if (c == 3) { STAMP(7); CSTAMP(27); }
            if (c == 5) { STAMP(9); CSTAMP(28); }
            if (c == 6) { STAMP(10); }
            __builtin_amdgcn_sched_barrier(0);
#pragma unroll
            for (int i = 0; i < 8; ++i) {
                const int kk = i >> 2, j = i & 3; const u32x4 xx = fx[c][2 * kk + (j & 1)], ww = fw[c & 1][2 * kk + (j >> 1)];
                if (j < 2) ak[j] = mfma16(ww, xx, ak[j]); else av[j - 2] = mfma16(xx, ww, av[j - 2]);
                if (c < 7 && i < 4) { frag(c + 1, 2 * i); frag(c + 1, 2 * i + 1); }
                if (c < 4 && (i & 1) == 0 && i < 6) issue(c + 4, i >> 1);
                if ((c == 4 || c == 5) && (i & 3) == 1) { const int n = (2 * (c - 4) + (i >> 2)) ^ (PFQ ? 0 : rot & 3);
                    dma16(wb + (64 * w + 16 * n) * 3072, (lane >> 2) * 3072 + (((lane & 3) ^ ((lane >> 4) & 2)) << 4), lds0 + L_WQ + (4 * w + n) * 1024); }
                __builtin_amdgcn_sched_barrier(0);
            }
        }
        kf = pack_pair(ak[0], ak[1]); vc = pack_pair(av[0], av[1]);
        asm volatile("s_waitcnt vmcnt(0)\n\ts_barrier" : "+v"(kf), "+v"(vc) :: "memory");
        __builtin_amdgcn_raw_buffer_store_b128(kf, rs_kv, lane * 16, tb * 8192, 16);
        __builtin_amdgcn_raw_buffer_store_b128(vc, rs_kv, lane * 16 + 1024, tb * 8192, 16);
        STAMP(12); CSTAMP(29);
        u32x4 fq[8][2];
        auto fragq = [&](int c) { const LAS char* s = smem + L_WQ + wa + chunk(c) * 4096; fq[c][0] = trd2(s, 256); fq[c][1] = trd2(s + 2048, 256); };
        fragq(0); fragq(1); fragq(2);
        __builtin_amdgcn_sched_barrier(0);
#pragma unroll
        for (int c = 0; c < 8; ++c) {
            aq[0] = mfma16(fq[c][0], fx[c][0], aq[0]); aq[1] = mfma16(fq[c][0], fx[c][1], aq[1]);
            if (c + 3 < 8) fragq(c + 3);
            __builtin_amdgcn_sched_barrier(0);
            aq[0] = mfma16(fq[c][1], fx[c][2], aq[0]); aq[1] = mfma16(fq[c][1], fx[c][3], aq[1]);
            __builtin_amdgcn_sched_barrier(0);
        }
        constexpr float QS = 0.25f * 1.4426950408889634f;
        qf = pack_pair(aq[0] * QS, aq[1] * QS);
        *(LAS u32x4*)(smem + L_Q + w * 1024 + lane * 16) = qf;
        __builtin_amdgcn_sched_barrier(0);
    }
    STAMP(4);

    const unsigned pat = ((lane & 11) == 0) ? 0x3f803f80u : 0u;
    u32x4 xv0, xv1;
#pragma unroll
    for (int j = 0; j < 4; ++j) { auto r = __builtin_amdgcn_permlane16_swap(vc[j], pat, false, false); xv0[j] = r[0]; xv1[j] = r[1]; }
    f32x16 o0 = {}, o1 = {}, o2 = {}, o3 = {};
    const int idx = 0xdddd;
    const char* kvf = ws + WS_KVF + hp * 2048;
    asm volatile("" : "+v"(o0), "+v"(o1), "+v"(o2), "+v"(o3), "+v"(xv0), "+v"(xv1));
    __builtin_amdgcn_s_waitcnt(0x0070);
    asm volatile("s_waitcnt vmcnt(0) lgkmcnt(0)\n\ts_barrier" ::: "memory");
    STAMP(13);
    if (tid == 0) __hip_atomic_store((GAS u64*)(kvf + tb * 128), tag, RLX_AGENT);
    u32x4 rg0 = __builtin_amdgcn_raw_buffer_load_b128(rs_rg, lane * 32, 0, 16), rg1 = __builtin_amdgcn_raw_buffer_load_b128(rs_rg, lane * 32 + 16, 0, 16);
    __builtin_amdgcn_sched_barrier(0);
    const u32x4 wgv = *(const u32x4*)(p.wg + (size_t)frow * 1792 + 64 * (fi >> 3) + 8 * (lane & 7));
    const u32x4 wuv = *(const u32x4*)(p.wu + (size_t)frow * 1792 + 64 * (fi >> 3) + 8 * (lane & 7));
    __builtin_amdgcn_sched_barrier(0);
    const u32x4 q0 = qf;
    const u32x4 q1 = *(const LAS u32x4*)(smem + L_Q + (hh * 4 + ((kq + 1) & 3)) * 1024 + lane * 16);
    const u32x4 q2 = *(const LAS u32x4*)(smem + L_Q + (hh * 4 + ((kq + 2) & 3)) * 1024 + lane * 16);
    const u32x4 q3 = *(const LAS u32x4*)(smem + L_Q + (hh * 4 + ((kq + 3) & 3)) * 1024 + lane * 16);
    asm volatile(
        "s_nop 1\n\t" QKM(SA, "%[k]", "%[q0]") "s_nop 11\n\t" EX(192) EX(193)
        UNIT_A("%[o0]", "%[v0]", "%[v1]", QKM(SB, "%[k]", "%[q1]"))
        UNIT_B("%[o1]", "%[v0]", "%[v1]", QKM(SA, "%[k]", "%[q2]"))
        UNIT_A("%[o2]", "%[v0]", "%[v1]", QKM(SB, "%[k]", "%[q3]"))
        UNIT_B_LAST("%[o3]", "%[v0]", "%[v1]") "s_nop 11"
        : [o0] "+v"(o0), [o1] "+v"(o1), [o2] "+v"(o2), [o3] "+v"(o3)
        : [k] "v"(kf), [v0] "v"(xv0), [v1] "v"(xv1), [q0] "v"(q0), [q1] "v"(q1), [q2] "v"(q2), [q3] "v"(q3), [idx] "v"(idx)
        : S_CLOB);
    asm volatile("" : "+v"(rg0), "+v"(rg1));
    STAMP(15);
    { const unsigned tl = (unsigned)tag, th = (unsigned)(tag >> 32) & 31u;
      auto fresh = [&]() { return __all((((rg0[0] ^ tl) | (rg0[2] ^ tl) | (rg1[0] ^ tl) | (rg1[2] ^ tl)) | (((rg0[1] ^ th) | (rg0[3] ^ th) | (rg1[1] ^ th) | (rg1[3] ^ th)) & 31u)) == 0); };
      if (!fresh()) do {
          --spin; asm volatile("" ::: "memory");
          rg0 = __builtin_amdgcn_raw_buffer_load_b128(rs_rg, lane * 32, 0, 16); rg1 = __builtin_amdgcn_raw_buffer_load_b128(rs_rg, lane * 32 + 16, 0, 16);
      } while (!fresh() && spin > 0); }
    int sel; float rsel;
    { const unsigned k0 = max(max(rg0[1], rg0[3]), max(rg1[1], rg1[3])) >> 5;
      const unsigned kk = __builtin_amdgcn_readfirstlane(wave_maxu(k0));
      sel = 2047 - (int)(kk & 2047u);
      const unsigned mono = kk >> 11, rb = (mono & 0x8000u) ? (mono & 0x7fffu) : (~mono & 0xffffu);
      rsel = bfr(sigmoidf_(bflo(rb))); }
    const unsigned xs = p.x[(size_t)sel * 512 + frow];
    if (blockIdx.x == 0 && tid == 0) __hip_atomic_store((GAS u64*)(ws + WS_EP), tag, RLX_AGENT);
    STAMP(14);

    volatile LAS unsigned* km = (volatile LAS unsigned*)(smem + L_KM);
    { const unsigned need = (15u << (tb & 12)) & ~(1u << tb);
      for (;;) {
          unsigned m;
          if (w == 0) { m = (unsigned)__ballot(__hip_atomic_load((GAS u64*)(kvf + t16 * 128), RLX_AGENT) == tag) & 0xffffu; km[0] = m; }
          else { m = __builtin_amdgcn_readfirstlane(km[0]); __builtin_amdgcn_s_sleep(1); }
          if ((m & need) == need || spin <= 0) break;
          --spin; }
      asm volatile("" :: "v"(xs), "v"(wgv), "v"(wuv) : "memory"); }
    STAMP(2);
    u32x4 yk, yv0, yv1, zk, zv0, zv1, uk, uv0, uv1;
    yv0 = yv1 = zv0 = zv1 = uv0 = uv1 = (u32x4){pat, pat, pat, pat};
    { const int s1 = (tb ^ 1) * 8192, s2 = (tb ^ 2) * 8192, s3 = (tb ^ 3) * 8192;
      yk = __builtin_amdgcn_raw_buffer_load_b128(rs_kv, lane * 16, s1, 16);
      if ((lane & 16) == 0) {
          yv0 = __builtin_amdgcn_raw_buffer_load_b128(rs_kv, lane * 16 + 1024, s1, 16); yv1 = __builtin_amdgcn_raw_buffer_load_b128(rs_kv, lane * 16 + 1280, s1, 16);
          zv0 = __builtin_amdgcn_raw_buffer_load_b128(rs_kv, lane * 16 + 1024, s2, 16); zv1 = __builtin_amdgcn_raw_buffer_load_b128(rs_kv, lane * 16 + 1280, s2, 16);
          uv0 = __builtin_amdgcn_raw_buffer_load_b128(rs_kv, lane * 16 + 1024, s3, 16); uv1 = __builtin_amdgcn_raw_buffer_load_b128(rs_kv, lane * 16 + 1280, s3, 16); }
      zk = __builtin_amdgcn_raw_buffer_load_b128(rs_kv, lane * 16, s2, 16);
      uk = __builtin_amdgcn_raw_buffer_load_b128(rs_kv, lane * 16, s3, 16); }
    u64 fl = 0;
    if (w == 0) fl = __hip_atomic_load((GAS u64*)(kvf + t16 * 128), RLX_AGENT);
    float ps = 0.f;
    {
        const float xf = bflo(xs);
        float a[16];
#pragma unroll
        for (int j = 0; j < 4; ++j) { a[2 * j] = xf * bflo(wgv[j]); a[2 * j + 1] = xf * bfhi(wgv[j]); a[8 + 2 * j] = xf * bflo(wuv[j]); a[9 + 2 * j] = xf * bfhi(wuv[j]); }
#pragma unroll
        for (int j = 0; j < 8; ++j) { auto r = __builtin_amdgcn_permlane32_swap(__float_as_uint(a[j]), __float_as_uint(a[j + 8]), false, false); a[j] = __uint_as_float(r[0]) + __uint_as_float(r[1]); }
#pragma unroll
        for (int j = 0; j < 4; ++j) { auto r = __builtin_amdgcn_permlane16_swap(__float_as_uint(a[j]), __float_as_uint(a[j + 4]), false, false); a[j] = __uint_as_float(r[0]) + __uint_as_float(r[1]); }
        const f32x4 sm = {a[0] + dppf<0x128>(a[0]), a[1] + dppf<0x128>(a[1]), a[2] + dppf<0x128>(a[2]), a[3] + dppf<0x128>(a[3])};
        if ((lane & 8) == 0) *(LAS f32x4*)(smem + L_FS + w * 512 + (lane >> 5) * 256 + (lane & 7) * 32 + (g4 & 1) * 16) = sm;
        if (w == 0) km[0] = (unsigned)__ballot(fl == tag) & 0xffffu;
        lds_bar();
        if (w < 2) {
#pragma unroll
            for (int i = 0; i < 8; ++i) ps += *(const LAS float*)(smem + L_FS + i * 512 + tid * 4); }
    }
    STAMP(8);
    asm volatile("" :: "v"(yk), "v"(yv0), "v"(yv1), "v"(zk), "v"(zv0), "v"(zv1), "v"(uk), "v"(uv0), "v"(uv1), "v"(fl) : "memory");
    if (lane == 0) __hip_atomic_store((GAS u64*)(ws + WS_G) + tokr, ((u64)gbits << 48) | (tag & ((1ull << 48) - 1)), RLX_AGENT);
    if (ffn && w < 2) __hip_atomic_store((GAS u64*)(ws + WS_P1) + fi * 128 + tid, ((u64)(unsigned)tag << 32) | __float_as_uint(ps), RLX_AGENT);
    const int tb3 = 2 * xcd + (jj & 1), cg3 = jj >> 1;
    STAMP(16);
    CSTAMP(30);
    { int gi = 0, cnt = 4, sb, t, u;
      const u64 vm = 0x0000ffff0000ffffull;
      int rdy = __builtin_amdgcn_readfirstlane(km[0]), m0s;
      unsigned wl = lds0 + L_WO + w * 4096, wv = (64 * w + (lane >> 2)) * 1024 + (((lane & 3) ^ ((lane >> 3) & 2)) << 4);
      asm volatile(
          "s_mov_b32 %[m0s], m0\n\ts_setprio 1\n\t" QKM(SA, "%[yk]", "%[q0]") "s_nop 11\n\t" EX(192) EX(193)
          "1:\n\t"
          TILE("y", "z", GROUP LOADB("0", "x"))
          TILE("z", "u", WODMA LOADB("0x2000", "y"))
          TILE("u", "x", LOADB("0x4000", "z"))
          "s_sub_u32 %[cnt], %[cnt], 1\n\ts_cmp_eq_u32 %[cnt], 0\n\ts_cbranch_scc1 4f\n\t"
          TILE("x", "y", LOADB("0x6000", "u"))
          "s_branch 1b\n\t"
          "4:\n\ts_mov_b32 m0, %[m0s]\n\ts_setprio 0\n\ts_waitcnt vmcnt(0)\n\ts_nop 11"
          : [o0] "+v"(o0), [o1] "+v"(o1), [o2] "+v"(o2), [o3] "+v"(o3),
            [xk] "+v"(kf), [xv0] "+v"(xv0), [xv1] "+v"(xv1), [yk] "+v"(yk), [yv0] "+v"(yv0), [yv1] "+v"(yv1),
            [zk] "+v"(zk), [zv0] "+v"(zv0), [zv1] "+v"(zv1), [uk] "+v"(uk), [uv0] "+v"(uv0), [uv1] "+v"(uv1),
            [gi] "+s"(gi), [cnt] "+s"(cnt), [rdy] "+s"(rdy), [spin] "+s"(spin),
            [sb] "=&s"(sb), [t] "=&s"(t), [u] "=&s"(u), [fl] "+v"(fl), [m0s] "=&s"(m0s), [wl] "+s"(wl), [wv] "+v"(wv)
          : [q0] "v"(q0), [q1] "v"(q1), [q2] "v"(q2), [q3] "v"(q3), [idx] "v"(idx), [ko] "v"(lane * 16),
            [rs] "s"(rs_kv), [vm] "s"(vm), [fo] "v"(t16 * 128), [fb] "s"(kvf), [tag] "s"(tag), [so] "s"(tb * 8192),
            [wp] "s"((const char*)p.wo + cg3 * 64)
          : S_CLOB, "vcc", "scc", "memory"); }
    STAMP(17);
    CSTAMP(31);
    const bool own = (sel >> 7) == tb3;
    const __amdgpu_buffer_rsrc_t rs_y = __builtin_amdgcn_make_buffer_rsrc(ws + WS_YP + (size_t)w * 28 * 4096, 0, 28 * 4096, 0x00020000);
    u32x4 yp[7];
    auto loady = [&]() {
#pragma unroll
        for (int i = 0; i < 7; ++i) yp[i] = __builtin_amdgcn_raw_buffer_load_b128(rs_y, g4 * 4096 + cg3 * 256 + t16 * 16, i * 16384, 16); };
    const int tok = 128 * tb3 + 16 * w + t16;
    const GAS u64* gp = (const GAS u64*)(ws + WS_G) + tok;
    u64 gg = __hip_atomic_load(gp, RLX_AGENT);
    const u32x2 xa = *(const u32x2*)(p.x + (size_t)tok * 512 + 32 * cg3 + 4 * g4), xc = *(const u32x2*)(p.x + (size_t)tok * 512 + 32 * cg3 + 16 + 4 * g4);
    { auto put = [&](const f32x16& o, int j) {
          LAS char* d = smem + L_CB + ((hh * 4 + ((kq + j) & 3)) * 3 + (j - 1)) * 2304;
          *(LAS f32x4*)(d + lane * 16) = (f32x4){o[0], o[1], o[2], o[3]};
          *(LAS f32x4*)(d + 1024 + lane * 16) = (f32x4){o[4], o[5], o[6], o[7]};
          *(LAS float*)(d + 2048 + lane * 4) = o[8]; };
      put(o1, 1); put(o2, 2); put(o3, 3); }
    {
        unsigned tk = 0;
        if (lane == 0) tk = __hip_atomic_fetch_add((LAS unsigned*)(smem + L_KM + 8), 1u, __ATOMIC_RELAXED, __HIP_MEMORY_SCOPE_WORKGROUP);
        tk = __builtin_amdgcn_readfirstlane(tk);
        if (ffn && tk < 4) {
            const __amdgpu_buffer_rsrc_t rs_p = __builtin_amdgcn_make_buffer_rsrc(ws + WS_P1 + (size_t)(fi >> 3) * 8192, 0, 8192, 0x00020000);
            const int po = (lane >> 3) * 1024 + ((lane >> 2) & 1) * 512 + (fi & 7) * 64 + (lane & 3) * 16;
            u32x4 pv = __builtin_amdgcn_raw_buffer_load_b128(rs_p, po, 0, 16);
            unsigned wd2[8];
#pragma unroll
            for (int f = 0; f < 8; ++f) wd2[f] = *(const unsigned*)(p.wd + (size_t)(8 * fi + f) * 512 + 128 * tk + 2 * lane);
            while (!__all(pv[1] == (unsigned)tag && pv[3] == (unsigned)tag) && spin > 0) {
                --spin; asm volatile("" ::: "memory"); pv = __builtin_amdgcn_raw_buffer_load_b128(rs_p, po, 0, 16); }
            float hv[2];
#pragma unroll
            for (int j = 0; j < 2; ++j) {
                float v = __uint_as_float(pv[2 * j]); v += dppf<0x128>(v);
                auto a = __builtin_amdgcn_permlane16_swap(__float_as_uint(v), __float_as_uint(v), false, false); v = __uint_as_float(a[0]) + __uint_as_float(a[1]);
                auto b = __builtin_amdgcn_permlane32_swap(__float_as_uint(v), __float_as_uint(v), false, false);
                const float gs = bfr(__uint_as_float(b[0]) + __uint_as_float(b[1])), us = dppf<0x124>(gs);
                hv[j] = bfr(bfr(gs * sigmoidf_(gs)) * us); }
            float y0 = 0.f, y1 = 0.f;
#pragma unroll
            for (int f = 0; f < 8; ++f) {
                const float h = __int_as_float(__builtin_amdgcn_readlane(__float_as_int(hv[f & 1]), f >> 1));
                y0 = fmaf(h, bflo(wd2[f]), y0); y1 = fmaf(h, bfhi(wd2[f]), y1); }
            const __amdgpu_buffer_rsrc_t rs_w = __builtin_amdgcn_make_buffer_rsrc(ws + WS_YP + (size_t)fi * 4096, 0, 4096, 0x00020000);
            __builtin_amdgcn_raw_buffer_store_b128((u32x4){__float_as_uint(y0), (unsigned)tag, __float_as_uint(y1), (unsigned)tag}, rs_w, lane * 16, tk * 1024, 16);
        } }
    lds_bar();
    if (own) loady();
    STAMP(18);
    float o[9];
#pragma unroll
    for (int r = 0; r < 9; ++r) o[r] = o0[r];
#pragma unroll
    for (int j = 0; j < 3; ++j) {
        const LAS char* d = smem + L_CB + (w * 3 + j) * 2304;
        const f32x4 a = *(const LAS f32x4*)(d + lane * 16), b = *(const LAS f32x4*)(d + 1024 + lane * 16);
#pragma unroll
        for (int r = 0; r < 4; ++r) { o[r] += a[r]; o[4 + r] += b[r]; }
        o[8] += *(const LAS float*)(d + 2048 + lane * 4);
    }
    { float mag = 0.f;
#pragma unroll
      for (int r = 0; r < 8; ++r) mag += fabsf(o[r]);
      const bool safe = o[8] >= 9.313225746154785e-10f && o[8] <= 1.8446744073709552e19f && mag <= 3.0e38f;
      if (!__all(safe)) {
          f32x16 acc = {}; float m = -INFINITY;
          u32x4 v0 = {pat, pat, pat, pat}, v1 = v0;
          const __amdgpu_buffer_rsrc_t rs_all = __builtin_amdgcn_make_buffer_rsrc(ws + WS_KV + (size_t)head * 131072, 0, 131072, 0x00020000);
          for (int t = 0; t < 64; ++t) {
              const u32x4 kk = __builtin_amdgcn_raw_buffer_load_b128(rs_all, lane * 16, t * 2048, 16);
              if ((lane & 16) == 0) { v0 = __builtin_amdgcn_raw_buffer_load_b128(rs_all, lane * 16 + 1024, t * 2048, 16); v1 = __builtin_amdgcn_raw_buffer_load_b128(rs_all, lane * 16 + 1280, t * 2048, 16); }
              f32x16 s = __builtin_amdgcn_mfma_f32_32x32x16_bf16(__builtin_bit_cast(bf16x8, kk), __builtin_bit_cast(bf16x8, q0), (f32x16){}, 0, 0, 0);
              float tm = s[0];
#pragma unroll
              for (int r = 1; r < 16; ++r) tm = fmaxf(tm, s[r]);
              auto sw = __builtin_amdgcn_permlane32_swap(__float_as_uint(tm), __float_as_uint(tm), false, false);
              const float mn = fmaxf(m, fmaxf(__uint_as_float(sw[0]), __uint_as_float(sw[1])));
              const float al = __builtin_amdgcn_exp2f(m - mn); m = mn;
#pragma unroll
              for (int r = 0; r < 16; ++r) { s[r] = __builtin_amdgcn_exp2f(s[r] - mn); acc[r] *= al; }
              acc = __builtin_amdgcn_smfmac_f32_32x32x32_bf16(__builtin_bit_cast(bf16x8, v0), __builtin_bit_cast(bf16x16, __builtin_shufflevector(s, s, 0, 1, 2, 3, 4, 5, 6, 7)), acc, idx, 0, 0);
              acc = __builtin_amdgcn_smfmac_f32_32x32x32_bf16(__builtin_bit_cast(bf16x8, v1), __builtin_bit_cast(bf16x16, __builtin_shufflevector(s, s, 8, 9, 10, 11, 12, 13, 14, 15)), acc, idx, 0, 0);
          }
#pragma unroll
          for (int r = 0; r < 9; ++r) o[r] = acc[r];
      } }
    { const float inv = __builtin_amdgcn_rcpf(o[8]);
      const u32x4 of = {cvtpk(o[0] * inv, o[1] * inv), cvtpk(o[2] * inv, o[3] * inv), cvtpk(o[4] * inv, o[5] * inv), cvtpk(o[6] * inv, o[7] * inv)};
      __builtin_amdgcn_raw_buffer_store_b128(of, rs_os, lane * 16, 0, 16); }
    __builtin_amdgcn_s_waitcnt(0x0f70);
    asm volatile("s_waitcnt vmcnt(0) lgkmcnt(0)\n\ts_barrier" ::: "memory");
    if (tid == 0) __hip_atomic_store((GAS u64*)(ws + WS_OF + (tb * 16 + hp) * 128), tag, RLX_AGENT);
    STAMP(19);

    if (own) {
        auto fresh = [&]() { bool ok = true;
#pragma unroll
            for (int i = 0; i < 7; ++i) ok = ok & (yp[i][1] == (unsigned)tag) & (yp[i][3] == (unsigned)tag);
            return __all(ok); };
        if (!fresh()) do { --spin; asm volatile("" ::: "memory"); loady(); } while (!fresh() && spin > 0);
        float s0 = 0.f, s1 = 0.f;
#pragma unroll
        for (int i = 0; i < 7; ++i) { s0 += __uint_as_float(yp[i][0]); s1 += __uint_as_float(yp[i][2]); }
        auto a = __builtin_amdgcn_permlane16_swap(__float_as_uint(s0), __float_as_uint(s1), false, false);
        const float v = __uint_as_float(a[0]) + __uint_as_float(a[1]);
        auto b = __builtin_amdgcn_permlane32_swap(__float_as_uint(v), __float_as_uint(v), false, false);
        if (lane < 32) *(LAS float*)(smem + L_YS + w * 128 + t16 * 8 + g4 * 4) = __uint_as_float(b[0]) + __uint_as_float(b[1]);
        lds_bar();
    }
    while (__any((gg ^ tag) << 16 != 0) && spin > 0) { --spin; gg = __hip_atomic_load(gp, RLX_AGENT); }
    const float gt = bfhi((unsigned)(gg >> 32));
    float bb[8] = {bflo(xa[0]), bfhi(xa[0]), bflo(xa[1]), bfhi(xa[1]), bflo(xc[0]), bfhi(xc[0]), bflo(xc[1]), bfhi(xc[1])};
    if (own && tok == sel) {
        f32x4 y0 = {}, y1 = {};
#pragma unroll
        for (int i = 0; i < 8; ++i) { y0 += *(const LAS f32x4*)(smem + L_YS + i * 128 + g4 * 16); y1 += *(const LAS f32x4*)(smem + L_YS + i * 128 + 64 + g4 * 16); }
#pragma unroll
        for (int r = 0; r < 4; ++r) { bb[r] = bfr(bb[r] + bfr(bfr(y0[r]) * rsel)); bb[4 + r] = bfr(bb[4 + r] + bfr(bfr(y1[r]) * rsel)); }
    }
    const float gm = bfr(1.0f - gt);
#pragma unroll
    for (int r = 0; r < 8; ++r) { bb[r] = bfr(gm * bb[r]); asm volatile("" : "+v"(bb[r])); }
    const __amdgpu_buffer_rsrc_t rs_o = __builtin_amdgcn_make_buffer_rsrc(ws + WS_O + (size_t)(4 * tb3 + (w >> 1)) * 32768, 0, 32768, 0x00020000);
    const int oo = (g4 >> 1) * 1024 + ((g4 & 1) * 32 + 16 * (w & 1) + t16) * 16;
    u32x4 ob[16];
    const LAS char* wa = smem + L_WO + ((g4 >> 1) * 16 + (g4 & 1) * 4 + ((lane & 15) >> 2)) * 64 + (lane & 3) * 8;
    f32x4 cg[4][2] = {};
    auto loadg = [&](int g) {
#pragma unroll
        for (int i = 4 * g; i < 4 * g + 4; ++i) { ob[i] = __builtin_amdgcn_raw_buffer_load_b128(rs_o, oo, i * 2048, 16); __builtin_amdgcn_sched_barrier(0); } };
    auto mulg = [&](int g) {
        const int h0 = (g4 & 1) << 5, h1 = h0 ^ 32; u32x4 wf[8];
#pragma unroll
        for (int i = 0; i < 4; ++i) { wf[2 * i] = trd2(wa + (4 * g + i) * 2048 + h0, 512); wf[2 * i + 1] = trd2(wa + (4 * g + i) * 2048 + h1, 512); }
        __builtin_amdgcn_sched_barrier(0);
#pragma unroll
        for (int i = 0; i < 4; ++i) { cg[g][0] = mfma16(wf[2 * i], ob[4 * g + i], cg[g][0]); cg[g][1] = mfma16(wf[2 * i + 1], ob[4 * g + i], cg[g][1]); }
        __builtin_amdgcn_sched_barrier(0); };
    const GAS u64* ofp = (const GAS u64*)(ws + WS_OF + (tb3 * 16 + t16) * 128);
    unsigned todo = 15;
    do {
        unsigned have;
        if (w == 0) { have = (unsigned)__ballot(__hip_atomic_load(ofp, RLX_AGENT) == tag); km[1] = have; }
        else { have = __builtin_amdgcn_readfirstlane(km[1]); __builtin_amdgcn_s_sleep(1); }
        if (spin <= 0) have = ~0u;
        --spin; asm volatile("" ::: "memory");
        if (todo == 15 && have == ~0u) {
            loadg(0); loadg(1); loadg(2); loadg(3); mulg(0); mulg(1); mulg(2); mulg(3);
            break; }
        unsigned go = 0;
#pragma unroll
        for (int g = 0; g < 4; ++g) if ((todo >> g & 1) && ((have >> (4 * g)) & 15u) == 15u) { go |= 1u << g; loadg(g); }
#pragma unroll
        for (int g = 0; g < 4; ++g) if (go >> g & 1) mulg(g);
        todo &= ~go;
    } while (todo);
    STAMP(20);
    STAMP(21);
    const f32x4 c0 = (cg[0][0] + cg[1][0]) + (cg[2][0] + cg[3][0]), c1 = (cg[0][1] + cg[1][1]) + (cg[2][1] + cg[3][1]);
    f32x4 r0, r1;
#pragma unroll
    for (int r = 0; r < 4; ++r) { r0[r] = bfr(gt * bfr(c0[r])) + bb[r]; r1[r] = bfr(gt * bfr(c1[r])) + bb[4 + r]; }
#define OAUX 0
    { const __amdgpu_buffer_rsrc_t rs_out = __builtin_amdgcn_make_buffer_rsrc(p.out, 0, 2u << 20, 0x00020000);
      __builtin_amdgcn_raw_buffer_store_b128(pack_pair(r0, r1), rs_out, tok * 1024 + 64 * cg3 + 32 * (g4 & 1) + 16 * (g4 >> 1), 0, OAUX); }
}

extern "C" void kernel_launch(void* const* d_in, const int* in_sizes, int n_in,
                              void* d_out, int out_size, void* d_ws, size_t ws_size,
                              hipStream_t stream) {
    Params p;
    p.x = (const uint16_t*)d_in[0]; p.wqkv = (const uint16_t*)d_in[1]; p.wo = (const uint16_t*)d_in[2];
    p.wr = (const uint16_t*)d_in[4]; p.wg = (const uint16_t*)d_in[5]; p.wu = (const uint16_t*)d_in[6];
    p.wd = (const uint16_t*)d_in[7]; p.gw = (const uint16_t*)d_in[8];
    p.out = (uint16_t*)d_out; p.ws = (char*)d_ws;
    mix_kernel<<<256, 512, 0, stream>>>(p);
}
```

```cpp
#include <hip/hip_runtime.h>
#include <hip/hip_bf16.h>
#include <stdint.h>

typedef __attribute__((ext_vector_type(8))) __bf16 bf16x8;
typedef __attribute__((ext_vector_type(16))) __bf16 bf16x16;
typedef __attribute__((ext_vector_type(4))) float f32x4;
typedef __attribute__((ext_vector_type(8))) float f32x8;
typedef __attribute__((ext_vector_type(16))) float f32x16;
typedef __attribute__((ext_vector_type(4))) unsigned u32x4;
typedef __attribute__((ext_vector_type(2))) unsigned u32x2;
typedef __attribute__((ext_vector_type(4))) short s16x4;
typedef unsigned long long u64;
#define LAS __attribute__((address_space(3)))
#define GAS __attribute__((address_space(1)))
#define RLX_AGENT __ATOMIC_RELAXED, __HIP_MEMORY_SCOPE_AGENT

constexpr size_t WS_KV = 0;
constexpr size_t WS_O = 4u << 20;
constexpr size_t WS_YP = 6u << 20;
constexpr size_t WS_G = 7u << 20;
constexpr size_t WS_RG = WS_G + 16384;
constexpr size_t WS_KVF = WS_RG + 2048;
constexpr size_t WS_OF = WS_KVF + 65536;
constexpr size_t WS_P1 = WS_OF + 65536;
constexpr size_t WS_EP = WS_P1 + 229376;

constexpr int SLOT = 24576, NSLOT = 5;
constexpr int L_WQ = SLOT * NSLOT;
constexpr int L_MISC = L_WQ + 32768;
constexpr int L_RK = L_MISC;
constexpr int L_W = L_MISC + 64;
constexpr int L_YS = L_MISC + 256;
constexpr int L_RW = L_MISC + 2048;
constexpr int LDS_BYTES = L_RW + 2048;
constexpr int L_CB = 0;
constexpr int L_Q = 57344;
constexpr int L_FS = 65536;
constexpr int L_EP = 69632;
constexpr int L_WO = 81920;
enum { W_Q = 0, W_KM = 2, W_C = 4, W_O = 6, W_F = 8, W_Y = 9, W_EM = 10, W_LM = 11, W_SEL = 12, W_LK = 13, W_NEAR = 14, W_RDY = 16 };
constexpr int SPIN_CAP = 200000;
#define POLL 0
#define KVAUX 16
#define OAUXL 16
#define PRIO2 2
#define PFQ 0
#define ROT 4

#define STAMP(i)
#define CSTAMP(i)
#define STAMP_DECL
#define STAMP_OUT

struct Params {
    const uint16_t* x; const uint16_t* wqkv; const uint16_t* wo; const uint16_t* wr;
    const uint16_t* wg; const uint16_t* wu; const uint16_t* wd; const uint16_t* gw;
    uint16_t* out; char* ws;
};

__device__ __forceinline__ float bflo(unsigned u) { return __uint_as_float(u << 16); }
__device__ __forceinline__ float bfhi(unsigned u) { return __uint_as_float(u & 0xffff0000u); }
__device__ __forceinline__ unsigned cvtpk(float lo, float hi) {
    typedef __attribute__((ext_vector_type(2))) float f2; typedef __attribute__((ext_vector_type(2))) __bf16 b2;
    return __builtin_bit_cast(unsigned, __builtin_convertvector((f2){lo, hi}, b2));
}
__device__ __forceinline__ float bfr(float v) { return bflo(cvtpk(v, 0.f) & 0xffffu); }
__device__ __forceinline__ float sigmoidf_(float v) { return __builtin_amdgcn_rcpf(1.0f + __builtin_amdgcn_exp2f(v * -1.4426950408889634f)); }
__device__ __forceinline__ f32x4 mfma16(u32x4 a, u32x4 b, f32x4 c) {
    return __builtin_amdgcn_mfma_f32_16x16x32_bf16(__builtin_bit_cast(bf16x8, a), __builtin_bit_cast(bf16x8, b), c, 0, 0, 0);
}
__device__ __forceinline__ u32x2 trd(const LAS char* p) {
    return __builtin_bit_cast(u32x2, __builtin_amdgcn_ds_read_tr16_b64_v4i16((LAS s16x4*)p));
}
__device__ __forceinline__ u32x4 trd2(const LAS char* p, int d) { u32x2 a = trd(p), b = trd(p + d); return (u32x4){a[0], a[1], b[0], b[1]}; }
__device__ __forceinline__ void dma16(const void* sbase, unsigned voff, unsigned lds) {
    unsigned keep;
    asm volatile("s_mov_b32 %0, m0\n\ts_mov_b32 m0, %3\n\ts_nop 0\n\tglobal_load_lds_dwordx4 %1, %2\n\ts_mov_b32 m0, %0"
                 : "=&s"(keep) : "v"(voff), "s"(sbase), "s"(lds) : "memory");
}
__device__ __forceinline__ void dma16_half(const void* sbase, unsigned voff, unsigned lds) {
    unsigned keep;
    asm volatile("s_mov_b32 %0, m0\n\ts_mov_b32 m0, %3\n\ts_mov_b32 exec_hi, 0\n\tglobal_load_lds_dwordx4 %1, %2\n\ts_mov_b32 exec_hi, -1\n\ts_mov_b32 m0, %0"
                 : "=&s"(keep) : "v"(voff), "s"(sbase), "s"(lds) : "memory");
}
template <int N> __device__ __forceinline__ void wait_bar() {
    asm volatile("" ::: "memory"); __builtin_amdgcn_s_waitcnt(0xc07f);
    asm volatile("s_waitcnt vmcnt(%0) lgkmcnt(0)\n\ts_barrier" :: "n"(N) : "memory"); }
template <int N> __device__ __forceinline__ void vm_bar() { asm volatile("s_waitcnt vmcnt(%0)\n\ts_barrier" :: "n"(N) : "memory"); }
__device__ __forceinline__ void lds_bar() { asm volatile("s_waitcnt lgkmcnt(0)\n\ts_barrier" ::: "memory"); }
__device__ __forceinline__ void bare_bar() { asm volatile("s_barrier" ::: "memory"); }
template <int CTRL> __device__ __forceinline__ float dppf(float v) {
    return __int_as_float(__builtin_amdgcn_update_dpp(0, __float_as_int(v), CTRL, 0xf, 0xf, true));
}
template <int CTRL> __device__ __forceinline__ unsigned dppu(unsigned v) {
    return (unsigned)__builtin_amdgcn_update_dpp(0, (int)v, CTRL, 0xf, 0xf, true);
}
__device__ __forceinline__ float row_sum16(float v) {
    v += dppf<0x128>(v); v += dppf<0x124>(v); v += dppf<0x122>(v); v += dppf<0x121>(v); return v;
}
__device__ __forceinline__ float wave_sum(float v) {
    v = row_sum16(v);
    auto a = __builtin_amdgcn_permlane16_swap(__float_as_uint(v), __float_as_uint(v), false, false);
    v = __uint_as_float(a[0]) + __uint_as_float(a[1]);
    auto b = __builtin_amdgcn_permlane32_swap(__float_as_uint(v), __float_as_uint(v), false, false);
    return __uint_as_float(b[0]) + __uint_as_float(b[1]);
}
__device__ __forceinline__ unsigned wave_maxu(unsigned v) {
    v = max(v, dppu<0x128>(v)); v = max(v, dppu<0x124>(v)); v = max(v, dppu<0x122>(v)); v = max(v, dppu<0x121>(v));
    auto a = __builtin_amdgcn_permlane16_swap(v, v, false, false); v = max(a[0], a[1]);
    auto b = __builtin_amdgcn_permlane32_swap(v, v, false, false); return max(b[0], b[1]);
}
__device__ __forceinline__ u32x4 pack_pair(f32x4 a, f32x4 b) {
    auto x = __builtin_amdgcn_permlane16_swap(cvtpk(a[0], a[1]), cvtpk(b[0], b[1]), false, false);
    auto y = __builtin_amdgcn_permlane16_swap(cvtpk(a[2], a[3]), cvtpk(b[2], b[3]), false, false);
    return (u32x4){x[0], y[0], x[1], y[1]};
}
#define STR2(x) #x
#define STR(x) STR2(x)
#define EX(r) "v_exp_f32 v" STR(r) ", v" STR(r) "\n\t"
#define QKM(S, K, Q) "v_mfma_f32_32x32x16_bf16 " S ", " K ", " Q ", 0\n\t"
#define PVM(O, V, P) "v_smfmac_f32_32x32x32_bf16 " O ", " V ", " P ", %[idx]\n\t"
#define UNIT_A(O, V0, V1, MID) EX(194) EX(195) EX(196) MID EX(197) EX(198) EX(199) EX(200) EX(201) PVM(O, V0, "v[192:199]") \
    EX(202) EX(203) EX(204) EX(205) EX(206) EX(207) EX(208) EX(209) PVM(O, V1, "v[200:207]")
#define UNIT_B(O, V0, V1, MID) EX(210) EX(211) EX(212) MID EX(213) EX(214) EX(215) EX(216) EX(217) PVM(O, V0, "v[208:215]") \
    EX(218) EX(219) EX(220) EX(221) EX(222) EX(223) EX(192) EX(193) PVM(O, V1, "v[216:223]")
#define UNIT_B_LAST(O, V0, V1) EX(210) EX(211) EX(212) EX(213) EX(214) EX(215) EX(216) EX(217) PVM(O, V0, "v[208:215]") \
    EX(218) EX(219) EX(220) EX(221) EX(222) EX(223) "s_nop 1\n\t" PVM(O, V1, "v[216:223]")
#define SA "v[192:207]"
#define SB "v[208:223]"
#define S_CLOB "v192", "v193", "v194", "v195", "v196", "v197", "v198", "v199", "v200", "v201", "v202", "v203", "v204", "v205", "v206", "v207", \
    "v208", "v209", "v210", "v211", "v212", "v213", "v214", "v215", "v216", "v217", "v218", "v219", "v220", "v221", "v222", "v223"
#define LOADT(J, K, V0, V1) "s_xor_b32 %[t], %[sb], " J "\n\t" \
    "buffer_load_dwordx4 " K ", %[ko], %[rs], %[t] offen sc1\n\t" \
    "s_mov_b64 exec, %[vm]\n\t" \
    "buffer_load_dwordx4 " V0 ", %[ko], %[rs], %[t] offen offset:1024 sc1\n\t" \
    "buffer_load_dwordx4 " V1 ", %[ko], %[rs], %[t] offen offset:1280 sc1\n\t" \
    "s_mov_b64 exec, -1\n\t"
#define GROUP "s_add_u32 %[gi], %[gi], 0x8000\n\ts_and_b32 %[gi], %[gi], 0x18000\n\ts_xor_b32 %[sb], %[so], %[gi]\n\t" \
    "s_lshr_b32 %[t], %[sb], 13\n\ts_and_b32 %[t], %[t], 12\n\ts_lshl_b32 %[t], 15, %[t]\n\t" \
    "s_andn2_b32 %[u], %[t], %[rdy]\n\ts_cbranch_scc0 3f\n\t" \
    "2:\n\ts_waitcnt vmcnt(0)\n\t" \
    "v_cmp_eq_u64 vcc, %[tag], %[fl]\n\ts_nop 4\n\ts_and_b32 %[rdy], vcc_lo, 0xffff\n\t" \
    "s_andn2_b32 %[u], %[t], %[rdy]\n\ts_cbranch_scc0 3f\n\t" \
    "global_load_dwordx2 %[fl], %[fo], %[fb] sc1\n\t" \
    "s_sub_i32 %[spin], %[spin], 1\n\ts_cmp_gt_i32 %[spin], 0\n\ts_cbranch_scc1 2b\n\t" \
    "3:\n\t"
#define TILE(B, BN, LD) \
    UNIT_A("%[o0]", "%[" B "v0]", "%[" B "v1]", QKM(SB, "%[" B "k]", "%[q1]") LD) \
    UNIT_B("%[o1]", "%[" B "v0]", "%[" B "v1]", QKM(SA, "%[" B "k]", "%[q2]")) \
    UNIT_A("%[o2]", "%[" B "v0]", "%[" B "v1]", QKM(SB, "%[" B "k]", "%[q3]")) \
    UNIT_B("%[o3]", "%[" B "v0]", "%[" B "v1]", "s_waitcnt vmcnt(6)\n\t" QKM(SA, "%[" BN "k]", "%[q0]"))
#define WODMA "s_cmp_lg_u32 %[hh], 0\n\ts_cbranch_scc1 5f\n\ts_mov_b32 m0, %[wl]\n\ts_add_u32 %[wl], %[wl], 0x400\n\t" \
    "global_load_lds_dwordx4 %[wv], %[wp]\n\tv_add_u32 %[wv], 0x4000, %[wv]\n\t5:\n\t"
#define LOADB(J, B) LOADT(J, "%[" B "k]", "%[" B "v0]", "%[" B "v1]")

__global__ __launch_bounds__(512, 2) void mix_kernel(Params p) {
    __shared__ __attribute__((aligned(16))) char smem_[LDS_BYTES];
    LAS char* smem = (LAS char*)smem_;
    const unsigned lds0 = (unsigned)(uintptr_t)smem;
    const int tid = threadIdx.x, lane = tid & 63;
    const int w = __builtin_amdgcn_readfirstlane(tid >> 6);
    const int xcd = blockIdx.x & 7, jj = blockIdx.x >> 3;
    const int tb = 4 * (xcd >> 1) + (jj & 3), hp = 8 * (xcd & 1) + (jj >> 2);
    const int kq = w & 3, hh = w >> 2, head = 2 * hp + hh;
    const int g4 = lane >> 4, t16 = lane & 15;
    char* const ws = p.ws;
    const __amdgpu_buffer_rsrc_t rs_kv = __builtin_amdgcn_make_buffer_rsrc(ws + WS_KV + (size_t)head * 131072 + kq * 2048, 0, 131072 - kq * 2048, 0x00020000);
    int spin = SPIN_CAP;
    STAMP_DECL; STAMP(0);
    asm volatile("" :: "s"(p.x), "s"(p.wqkv), "s"(p.wo), "s"(p.wr), "s"(p.wg), "s"(p.wu), "s"(p.wd), "s"(p.gw), "s"(p.out), "s"(p.ws));
    STAMP(24);

    const int tokr = 128 * tb + 8 * hp + w;
    u64 ep;
    asm volatile("s_load_dwordx2 %0, %1, 0x0" : "=s"(ep) : "s"(ws + WS_EP) : "memory");
    if (w < 2) dma16(w ? p.gw : p.wr, lane * 16, lds0 + L_RW + w * 1024);
    unsigned pq = 0;
    if (PFQ && w == 7) asm volatile("global_load_dword %0, %1, %2" : "=v"(pq) : "v"((jj * 16 + (lane >> 2)) * 3072 + (xcd & 1) * 512 + (lane & 3) * 128), "s"(p.wqkv) : "memory");
    u32x4 rx;
    asm volatile("global_load_dwordx4 %0, %1, %2" : "=v"(rx) : "v"(lane * 16), "s"(p.x + (size_t)tokr * 512) : "memory");
    unsigned xo0, xo1, wo0;
    { const int t0 = 16 * w + (lane >> 3), t1 = t0 + 8;
      xo0 = t0 * 1024 + (((lane & 7) ^ ((t0 >> 1) & 7)) << 4);
      xo1 = t1 * 1024 + (((lane & 7) ^ ((t1 >> 1) & 7)) << 4);
      const int k0 = 16 * (w & 3) + (lane >> 2), pc = ((lane & 3) ^ (((k0 >> 3) & 1) << 1)) << 4;
      wo0 = k0 * 3072 + 1024 + (w >> 2) * 1024 + pc; }
    const char* xb = (const char*)p.x + (size_t)tb * 131072;
    const char* wb = (const char*)p.wqkv + hp * 64;
    const int rot = ROT == 0 ? 0 : ROT == 1 ? (2 * (jj & 3) + (jj >> 2)) & 7 : ROT == 2 ? (((jj & 3) + (jj >> 2)) & 1) * 4 : ((jj & 3) + (jj >> 2) * 2 + (jj >> 3)) & 3;
    const int rx4 = ROT == 5 ? rot ^ (((xcd ^ (xcd >> 1)) & 1) << 2) : rot;
    auto chunk = [&](int c) { return ROT >= 4 ? c ^ rx4 : (c + rot) & 7; };
    auto issue = [&](int c, int i) {
        const unsigned s = lds0 + (c % NSLOT) * SLOT; const int cc = chunk(c);
        if (i == 0) dma16(xb + cc * 128, xo0, s + w * 2048);
        if (i == 1) dma16(wb + cc * 196608, wo0, s + 16384 + w * 1024);
        if (i == 2) dma16(xb + cc * 128, xo1, s + w * 2048 + 1024);
    };
    const __amdgpu_buffer_rsrc_t rs_os = __builtin_amdgcn_make_buffer_rsrc(ws + WS_O + (size_t)(4 * tb + kq) * 32768 + head * 1024, 0, 1024, 0x00020000);
#pragma unroll
    for (int i = 0; i < 3; ++i) issue(0, i);
    if (tid < 32) *(LAS unsigned*)(smem + L_W + 4 * tid) = 0u;
    asm volatile("s_waitcnt vmcnt(3) lgkmcnt(0)\n\ts_barrier" : "+v"(rx), "+s"(ep) : "v"(pq) : "memory");
    const u64 tag = ep + 1;
    unsigned gbits;
    {
            const u32x4 rw = *(const LAS u32x4*)(smem + L_RW + lane * 16), rgw = *(const LAS u32x4*)(smem + L_RW + 1024 + lane * 16);
            float ra = 0.f, ga = 0.f;
#pragma unroll
            for (int j = 0; j < 4; ++j) {
                const float a = bflo(rx[j]), b = bfhi(rx[j]);
                ra = fmaf(a, bflo(rw[j]), ra); ra = fmaf(b, bfhi(rw[j]), ra);
                const unsigned pr = cvtpk(a * bflo(rgw[j]), b * bfhi(rgw[j]));
                ga += bflo(pr); ga += bfhi(pr); }
            ra = wave_sum(ra); ga = wave_sum(ga);
            const unsigned rb = cvtpk(ra, 0.f) & 0xffffu;
            const unsigned key = (((rb & 0x8000u) ? (~rb & 0xffffu) : (rb | 0x8000u)) << 11) | (unsigned)(2047 - tokr);
            gbits = cvtpk(sigmoidf_(bfr(ga)), 0.f) & 0xffffu;
            if (lane == 0) *(LAS unsigned*)(smem + L_RK + 4 * w) = key; }
#pragma unroll
    for (int c = 1; c < 3; ++c) {
#pragma unroll
        for (int i = 0; i < 3; ++i) issue(c, i);
        if (c == 1) { lds_bar();
            if (w == 0) {
                const unsigned kk = wave_maxu(*(const LAS unsigned*)(smem + L_RK + 4 * (lane & 7)));
                if (lane == 0) __hip_atomic_store((GAS u64*)(ws + WS_RG) + blockIdx.x, ((u64)kk << 37) | (tag & ((1ull << 37) - 1)), RLX_AGENT);
            }
        }
    }
    const int fi = min((int)blockIdx.x, 223); const bool ffn = blockIdx.x < 224;
    const __amdgpu_buffer_rsrc_t rs_rg = __builtin_amdgcn_make_buffer_rsrc(ws + WS_RG, 0, 2048, 0x00020000);
    STAMP(1);
    u32x4 kf, vc, qf;
    const int frow = 64 * (fi & 7) + 16 * kq + (lane >> 3);
    {   f32x4 aq[2] = {}, ak[2] = {}, av[2] = {};
        const int fsw = (t16 >> 1) & 7;
        const int xa = (32 * kq + t16) * 128 + ((g4 ^ fsw) << 4);
        const int wa = g4 * 512 + ((lane & 15) >> 2) * 64 + ((hh ^ (g4 & 1)) << 5) + (lane & 3) * 8;
        u32x4 fx[8][4], fw[2][4];
        auto frag = [&](int c, int i) {
            const LAS char* s = smem + (c % NSLOT) * SLOT; const int kk = i >> 2, j = i & 3;
            if (j == 1 || j == 2) fx[c][2 * kk + j - 1] = *(const LAS u32x4*)(s + (xa ^ (kk * 64)) + (j - 1) * 2048);
            else fw[c & 1][2 * kk + (j ? 1 : 0)] = trd2(s + 16384 + wa + (j ? 4096 : 0) + kk * 2048, 256); };
        wait_bar<6>();
        STAMP(3); CSTAMP(24);
#pragma unroll
        for (int i = 0; i < 8; ++i) { frag(0, i); if (i < 3) issue(3, i); __builtin_amdgcn_sched_barrier(0); }
#pragma unroll
        for (int c = 0; c < 8; ++c) {
            if (c < 5) vm_bar<6>(); else if (c == 5) vm_bar<5>(); else if (c == 6) vm_bar<4>();
            if (c == 1) { STAMP(6); CSTAMP(25); }
            if (c == 2) { STAMP(11); CSTAMP(26); }
            if (c == 3) { STAMP(7); CSTAMP(27); }
            if (c == 5) { STAMP(9); CSTAMP(28); }
            if (c == 6) { STAMP(10); }
            __builtin_amdgcn_sched_barrier(0);
#pragma unroll
            for (int i = 0; i < 8; ++i) {
                const int kk = i >> 2, j = i & 3; const u32x4 xx = fx[c][2 * kk + (j & 1)], ww = fw[c & 1][2 * kk + (j >> 1)];
                if (j < 2) ak[j] = mfma16(ww, xx, ak[j]); else av[j - 2] = mfma16(xx, ww, av[j - 2]);
                if (c < 7 && i < 4) { frag(c + 1, 2 * i); frag(c + 1, 2 * i + 1); }
                if (c < 4 && (i & 1) == 0 && i < 6) issue(c + 4, i >> 1);
                if ((c == 4 || c == 5) && (i & 3) == 1) { const int n = (2 * (c - 4) + (i >> 2)) ^ (PFQ ? 0 : rot & 3);
                    dma16(wb + (64 * w + 16 * n) * 3072, (lane >> 2) * 3072 + (((lane & 3) ^ ((lane >> 4) & 2)) << 4), lds0 + L_WQ + (4 * w + n) * 1024); }
                __builtin_amdgcn_sched_barrier(0);
            }
        }
        kf = pack_pair(ak[0], ak[1]); vc = pack_pair(av[0], av[1]);
        asm volatile("s_waitcnt vmcnt(0)\n\ts_barrier" : "+v"(kf), "+v"(vc) :: "memory");
        __builtin_amdgcn_raw_buffer_store_b128(kf, rs_kv, lane * 16, tb * 8192, 16);
        __builtin_amdgcn_raw_buffer_store_b128(vc, rs_kv, lane * 16 + 1024, tb * 8192, 16);
        STAMP(12); CSTAMP(29);
        u32x4 fq[8][2];
        auto fragq = [&](int c) { const LAS char* s = smem + L_WQ + wa + chunk(c) * 4096; fq[c][0] = trd2(s, 256); fq[c][1] = trd2(s + 2048, 256); };
        fragq(0); fragq(1); fragq(2);
        __builtin_amdgcn_sched_barrier(0);
#pragma unroll
        for (int c = 0; c < 8; ++c) {
            aq[0] = mfma16(fq[c][0], fx[c][0], aq[0]); aq[1] = mfma16(fq[c][0], fx[c][1], aq[1]);
            if (c + 3 < 8) fragq(c + 3);
            __builtin_amdgcn_sched_barrier(0);
            aq[0] = mfma16(fq[c][1], fx[c][2], aq[0]); aq[1] = mfma16(fq[c][1], fx[c][3], aq[1]);
            __builtin_amdgcn_sched_barrier(0);
        }
        constexpr float QS = 0.25f * 1.4426950408889634f;
        qf = pack_pair(aq[0] * QS, aq[1] * QS);
        *(LAS u32x4*)(smem + L_Q + w * 1024 + lane * 16) = qf;
        __builtin_amdgcn_sched_barrier(0);
    }
    STAMP(4);

    volatile LAS unsigned* lw = (volatile LAS unsigned*)(smem + L_W);
    auto arrive = [&](int word) -> unsigned {
        unsigned t = 0; asm volatile("s_waitcnt lgkmcnt(0)" ::: "memory");
        if (lane == 0) t = __hip_atomic_fetch_add((LAS unsigned*)(smem + L_W) + word, 1u, __ATOMIC_RELAXED, __HIP_MEMORY_SCOPE_WORKGROUP);
        return __builtin_amdgcn_readfirstlane(t); };
    auto await = [&](int word, unsigned n) {
        while ((unsigned)__builtin_amdgcn_readfirstlane(lw[word]) < n && spin > 0) { --spin; __builtin_amdgcn_s_sleep(2); }
        asm volatile("" ::: "memory"); };
    const unsigned pat = ((lane & 11) == 0) ? 0x3f803f80u : 0u;
    u32x4 xv0, xv1;
#pragma unroll
    for (int j = 0; j < 4; ++j) { auto r = __builtin_amdgcn_permlane16_swap(vc[j], pat, false, false); xv0[j] = r[0]; xv1[j] = r[1]; }
    f32x16 o0 = {}, o1 = {}, o2 = {}, o3 = {};
    const int idx = 0xdddd;
    const char* kvf = ws + WS_KVF + head * 2048;
    asm volatile("" : "+v"(o0), "+v"(o1), "+v"(o2), "+v"(o3), "+v"(xv0), "+v"(xv1));
    __builtin_amdgcn_s_waitcnt(0x0070);
    asm volatile("s_waitcnt vmcnt(0)" ::: "memory");
    if (arrive(W_Q + hh) == 3 && lane == 0) asm volatile("global_store_dwordx2 %0, %1, %2 sc1" :: "v"(tb * 128), "v"(tag), "s"(kvf) : "memory");
    u32x4 rg0, rg1;
    if (w == 4) { rg0 = __builtin_amdgcn_raw_buffer_load_b128(rs_rg, lane * 32, 0, 16); rg1 = __builtin_amdgcn_raw_buffer_load_b128(rs_rg, lane * 32 + 16, 0, 16); }
    await(W_Q + hh, 4);
    STAMP(13);
    const u32x4 q0 = qf;
    const u32x4 q1 = *(const LAS u32x4*)(smem + L_Q + (hh * 4 + ((kq + 1) & 3)) * 1024 + lane * 16);
    const u32x4 q2 = *(const LAS u32x4*)(smem + L_Q + (hh * 4 + ((kq + 2) & 3)) * 1024 + lane * 16);
    const u32x4 q3 = *(const LAS u32x4*)(smem + L_Q + (hh * 4 + ((kq + 3) & 3)) * 1024 + lane * 16);
    asm volatile(
        "s_nop 1\n\t" QKM(SA, "%[k]", "%[q0]") "s_nop 11\n\t" EX(192) EX(193)
        UNIT_A("%[o0]", "%[v0]", "%[v1]", QKM(SB, "%[k]", "%[q1]"))
        UNIT_B("%[o1]", "%[v0]", "%[v1]", QKM(SA, "%[k]", "%[q2]"))
        UNIT_A("%[o2]", "%[v0]", "%[v1]", QKM(SB, "%[k]", "%[q3]"))
        UNIT_B_LAST("%[o3]", "%[v0]", "%[v1]") "s_nop 11"
        : [o0] "+v"(o0), [o1] "+v"(o1), [o2] "+v"(o2), [o3] "+v"(o3)
        : [k] "v"(kf), [v0] "v"(xv0), [v1] "v"(xv1), [q0] "v"(q0), [q1] "v"(q1), [q2] "v"(q2), [q3] "v"(q3), [idx] "v"(idx)
        : S_CLOB);
    STAMP(15);
    __builtin_amdgcn_s_setprio(2);
    if (w == 4) {
        const unsigned tl = (unsigned)tag, th = (unsigned)(tag >> 32) & 31u;
        auto fresh = [&]() { return __all((((rg0[0] ^ tl) | (rg0[2] ^ tl) | (rg1[0] ^ tl) | (rg1[2] ^ tl)) | (((rg0[1] ^ th) | (rg0[3] ^ th) | (rg1[1] ^ th) | (rg1[3] ^ th)) & 31u)) == 0); };
        if (!fresh()) do {
            --spin; asm volatile("" ::: "memory");
            rg0 = __builtin_amdgcn_raw_buffer_load_b128(rs_rg, lane * 32, 0, 16); rg1 = __builtin_amdgcn_raw_buffer_load_b128(rs_rg, lane * 32 + 16, 0, 16);
        } while (!fresh() && spin > 0);
        const unsigned kk = __builtin_amdgcn_readfirstlane(wave_maxu(max(max(rg0[1], rg0[3]), max(rg1[1], rg1[3])) >> 5));
        lw[W_SEL] = kk | 0x80000000u;
        if (blockIdx.x == 0 && lane == 0) __hip_atomic_store((GAS u64*)(ws + WS_EP), tag, RLX_AGENT);
    }
    { const unsigned need = (15u << (tb & 12)) & ~(1u << tb);
      for (;;) {
          unsigned m;
          if (kq == 0) { m = (unsigned)__ballot(__hip_atomic_load((GAS u64*)(kvf + t16 * 128), RLX_AGENT) == tag) & 0xffffu; lw[W_KM + hh] = m; }
          else { m = __builtin_amdgcn_readfirstlane(lw[W_KM + hh]); __builtin_amdgcn_s_sleep(1); }
          if ((m & need) == need || spin <= 0) break;
          --spin; }
      asm volatile("" ::: "memory"); }
    STAMP(2);
    u32x4 yk, yv0, yv1, zk, zv0, zv1, uk, uv0, uv1;
    yv0 = yv1 = zv0 = zv1 = uv0 = uv1 = (u32x4){pat, pat, pat, pat};
    { const int s1 = (tb ^ 1) * 8192, s2 = (tb ^ 2) * 8192, s3 = (tb ^ 3) * 8192;
      yk = __builtin_amdgcn_raw_buffer_load_b128(rs_kv, lane * 16, s1, KVAUX);
      if ((lane & 16) == 0) {
          yv0 = __builtin_amdgcn_raw_buffer_load_b128(rs_kv, lane * 16 + 1024, s1, KVAUX); yv1 = __builtin_amdgcn_raw_buffer_load_b128(rs_kv, lane * 16 + 1280, s1, KVAUX);
          zv0 = __builtin_amdgcn_raw_buffer_load_b128(rs_kv, lane * 16 + 1024, s2, KVAUX); zv1 = __builtin_amdgcn_raw_buffer_load_b128(rs_kv, lane * 16 + 1280, s2, KVAUX);
          uv0 = __builtin_amdgcn_raw_buffer_load_b128(rs_kv, lane * 16 + 1024, s3, KVAUX); uv1 = __builtin_amdgcn_raw_buffer_load_b128(rs_kv, lane * 16 + 1280, s3, KVAUX); }
      zk = __builtin_amdgcn_raw_buffer_load_b128(rs_kv, lane * 16, s2, KVAUX);
      uk = __builtin_amdgcn_raw_buffer_load_b128(rs_kv, lane * 16, s3, KVAUX); }
    u64 fl = __hip_atomic_load((GAS u64*)(kvf + t16 * 128), RLX_AGENT);
    if (hh == 1) {
        u32x4 wv4[4];
#pragma unroll
        for (int j = 0; j < 4; ++j) wv4[j] = *(const u32x4*)(((j & 1) ? p.wu : p.wg) + (size_t)(frow + 8 * (j >> 1)) * 1792 + 64 * (fi >> 3) + 8 * (lane & 7));
        await(W_SEL, 1u);
        const uint16_t* xr = p.x + (size_t)(2047 - (int)(__builtin_amdgcn_readfirstlane(lw[W_SEL]) & 2047u)) * 512 + frow;
        const float xf0 = bflo(xr[0]), xf1 = bflo(xr[8]);
        float a[16];
#pragma unroll
        for (int j = 0; j < 4; ++j) {
            a[2 * j] = fmaf(xf1, bflo(wv4[2][j]), xf0 * bflo(wv4[0][j])); a[2 * j + 1] = fmaf(xf1, bfhi(wv4[2][j]), xf0 * bfhi(wv4[0][j]));
            a[8 + 2 * j] = fmaf(xf1, bflo(wv4[3][j]), xf0 * bflo(wv4[1][j])); a[9 + 2 * j] = fmaf(xf1, bfhi(wv4[3][j]), xf0 * bfhi(wv4[1][j])); }
#pragma unroll
        for (int j = 0; j < 8; ++j) { auto r = __builtin_amdgcn_permlane32_swap(__float_as_uint(a[j]), __float_as_uint(a[j + 8]), false, false); a[j] = __uint_as_float(r[0]) + __uint_as_float(r[1]); }
#pragma unroll
        for (int j = 0; j < 4; ++j) { auto r = __builtin_amdgcn_permlane16_swap(__float_as_uint(a[j]), __float_as_uint(a[j + 4]), false, false); a[j] = __uint_as_float(r[0]) + __uint_as_float(r[1]); }
        const f32x4 sm = {a[0] + dppf<0x128>(a[0]), a[1] + dppf<0x128>(a[1]), a[2] + dppf<0x128>(a[2]), a[3] + dppf<0x128>(a[3])};
        if ((lane & 8) == 0) *(LAS f32x4*)(smem + L_FS + kq * 512 + (lane >> 5) * 256 + (lane & 7) * 32 + (g4 & 1) * 16) = sm;
        arrive(W_F);
        if (ffn && kq < 2) {
            await(W_F, 4);
            float ps = 0.f;
#pragma unroll
            for (int i = 0; i < 4; ++i) ps += *(const LAS float*)(smem + L_FS + i * 512 + (kq * 64 + lane) * 4);
            __hip_atomic_store((GAS u64*)(ws + WS_P1) + fi * 128 + kq * 64 + lane, ((u64)(unsigned)tag << 32) | __float_as_uint(ps), RLX_AGENT); }
    }
    STAMP(8);
    asm volatile("" :: "v"(yk), "v"(yv0), "v"(yv1), "v"(zk), "v"(zv0), "v"(zv1), "v"(uk), "v"(uv0), "v"(uv1), "v"(fl) : "memory");
    if (lane == 0) __hip_atomic_store((GAS u64*)(ws + WS_G) + tokr, ((u64)gbits << 48) | (tag & ((1ull << 48) - 1)), RLX_AGENT);
    const int tb3 = 2 * xcd + (jj & 1), cg3 = jj >> 1;
    STAMP(16);
    CSTAMP(30);
    { int gi = 0, cnt = 4, sb, t, u;
      const u64 vm = 0x0000ffff0000ffffull;
      int rdy = (int)(unsigned)__ballot(fl == tag) & 0xffff, m0s;
      unsigned wl = lds0 + L_WO + kq * 8192, wv = (128 * kq + (lane >> 2)) * 1024 + (((lane & 3) ^ ((lane >> 3) & 2)) << 4);
      asm volatile(
          "s_mov_b32 %[m0s], m0\n\ts_setprio 1\n\t" QKM(SA, "%[yk]", "%[q0]") "s_nop 11\n\t" EX(192) EX(193)
          "1:\n\t"
          TILE("y", "z", GROUP LOADB("0", "x"))
          TILE("z", "u", WODMA LOADB("0x2000", "y"))
          TILE("u", "x", WODMA LOADB("0x4000", "z"))
          "s_sub_u32 %[cnt], %[cnt], 1\n\ts_cmp_eq_u32 %[cnt], 0\n\ts_cbranch_scc1 4f\n\t"
          TILE("x", "y", LOADB("0x6000", "u"))
          "s_branch 1b\n\t"
          "4:\n\ts_mov_b32 m0, %[m0s]\n\ts_setprio 2\n\ts_waitcnt vmcnt(0)\n\ts_nop 11"
          : [o0] "+v"(o0), [o1] "+v"(o1), [o2] "+v"(o2), [o3] "+v"(o3),
            [xk] "+v"(kf), [xv0] "+v"(xv0), [xv1] "+v"(xv1), [yk] "+v"(yk), [yv0] "+v"(yv0), [yv1] "+v"(yv1),
            [zk] "+v"(zk), [zv0] "+v"(zv0), [zv1] "+v"(zv1), [uk] "+v"(uk), [uv0] "+v"(uv0), [uv1] "+v"(uv1),
            [gi] "+s"(gi), [cnt] "+s"(cnt), [rdy] "+s"(rdy), [spin] "+s"(spin),
            [sb] "=&s"(sb), [t] "=&s"(t), [u] "=&s"(u), [fl] "+v"(fl), [m0s] "=&s"(m0s), [wl] "+s"(wl), [wv] "+v"(wv)
          : [q0] "v"(q0), [q1] "v"(q1), [q2] "v"(q2), [q3] "v"(q3), [idx] "v"(idx), [ko] "v"(lane * 16),
            [rs] "s"(rs_kv), [vm] "s"(vm), [fo] "v"(t16 * 128), [fb] "s"(kvf), [tag] "s"(tag), [so] "s"(tb * 8192),
            [wp] "s"((const char*)p.wo + cg3 * 64), [hh] "s"(hh)
          : S_CLOB, "vcc", "scc", "memory"); }
    STAMP(17);
    if (hh == 1) lw[W_NEAR] = 1u;
    CSTAMP(31);
    const int tok = 128 * tb3 + 32 * kq + 16 * hh + t16;
    const GAS u64* gp = (const GAS u64*)(ws + WS_G) + tok;
    u64 gg = __hip_atomic_load(gp, RLX_AGENT);
    const u32x2 xa = *(const u32x2*)(p.x + (size_t)tok * 512 + 32 * cg3 + 4 * g4), xc = *(const u32x2*)(p.x + (size_t)tok * 512 + 32 * cg3 + 16 + 4 * g4);
    { auto put = [&](const f32x16& o, int j) {
          LAS char* d = smem + L_CB + ((hh * 4 + ((kq + j) & 3)) * 3 + (j - 1)) * 2304;
          *(LAS f32x4*)(d + lane * 16) = (f32x4){o[0], o[1], o[2], o[3]};
          *(LAS f32x4*)(d + 1024 + lane * 16) = (f32x4){o[4], o[5], o[6], o[7]};
          *(LAS float*)(d + 2048 + lane * 4) = o[8]; };
      put(o1, 1); put(o2, 2); put(o3, 3); }
    arrive(W_C + hh); await(W_C + hh, 4);
    STAMP(18);
    float o[9];
#pragma unroll
    for (int r = 0; r < 9; ++r) o[r] = o0[r];
#pragma unroll
    for (int j = 0; j < 3; ++j) {
        const LAS char* d = smem + L_CB + (w * 3 + j) * 2304;
        const f32x4 a = *(const LAS f32x4*)(d + lane * 16), b = *(const LAS f32x4*)(d + 1024 + lane * 16);
#pragma unroll
        for (int r = 0; r < 4; ++r) { o[r] += a[r]; o[4 + r] += b[r]; }
        o[8] += *(const LAS float*)(d + 2048 + lane * 4);
    }
    { float mag = 0.f;
#pragma unroll
      for (int r = 0; r < 8; ++r) mag += fabsf(o[r]);
      const bool safe = o[8] >= 9.313225746154785e-10f && o[8] <= 1.8446744073709552e19f && mag <= 3.0e38f;
      if (!__all(safe)) {
          f32x16 acc = {}; float m = -INFINITY;
          u32x4 v0 = {pat, pat, pat, pat}, v1 = v0;
          const __amdgpu_buffer_rsrc_t rs_all = __builtin_amdgcn_make_buffer_rsrc(ws + WS_KV + (size_t)head * 131072, 0, 131072, 0x00020000);
          for (int t = 0; t < 64; ++t) {
              const u32x4 kk = __builtin_amdgcn_raw_buffer_load_b128(rs_all, lane * 16, t * 2048, 16);
              if ((lane & 16) == 0) { v0 = __builtin_amdgcn_raw_buffer_load_b128(rs_all, lane * 16 + 1024, t * 2048, 16); v1 = __builtin_amdgcn_raw_buffer_load_b128(rs_all, lane * 16 + 1280, t * 2048, 16); }
              f32x16 s = __builtin_amdgcn_mfma_f32_32x32x16_bf16(__builtin_bit_cast(bf16x8, kk), __builtin_bit_cast(bf16x8, q0), (f32x16){}, 0, 0, 0);
              float tm = s[0];
#pragma unroll
              for (int r = 1; r < 16; ++r) tm = fmaxf(tm, s[r]);
              auto sw = __builtin_amdgcn_permlane32_swap(__float_as_uint(tm), __float_as_uint(tm), false, false);
              const float mn = fmaxf(m, fmaxf(__uint_as_float(sw[0]), __uint_as_float(sw[1])));
              const float al = __builtin_amdgcn_exp2f(m - mn); m = mn;
#pragma unroll
              for (int r = 0; r < 16; ++r) { s[r] = __builtin_amdgcn_exp2f(s[r] - mn); acc[r] *= al; }
              acc = __builtin_amdgcn_smfmac_f32_32x32x32_bf16(__builtin_bit_cast(bf16x8, v0), __builtin_bit_cast(bf16x16, __builtin_shufflevector(s, s, 0, 1, 2, 3, 4, 5, 6, 7)), acc, idx, 0, 0);
              acc = __builtin_amdgcn_smfmac_f32_32x32x32_bf16(__builtin_bit_cast(bf16x8, v1), __builtin_bit_cast(bf16x16, __builtin_shufflevector(s, s, 8, 9, 10, 11, 12, 13, 14, 15)), acc, idx, 0, 0);
          }
#pragma unroll
          for (int r = 0; r < 9; ++r) o[r] = acc[r];
      } }
    { const float inv = __builtin_amdgcn_rcpf(o[8]);
      const u32x4 of = {cvtpk(o[0] * inv, o[1] * inv), cvtpk(o[2] * inv, o[3] * inv), cvtpk(o[4] * inv, o[5] * inv), cvtpk(o[6] * inv, o[7] * inv)};
      __builtin_amdgcn_raw_buffer_store_b128(of, rs_os, lane * 16, 0, 16); }
    auto publish_o = [&](int younger) {
        if (younger) asm volatile("s_waitcnt vmcnt(8)" ::: "memory"); else asm volatile("s_waitcnt vmcnt(0)" ::: "memory");
        if (arrive(W_O + hh) == 3 && lane == 0)
            asm volatile("global_store_dwordx2 %0, %1, %2 sc1" :: "v"(((hh * 16 + tb) * 16 + hp) * 128), "v"(tag), "s"(ws + WS_OF) : "memory"); };

    const __amdgpu_buffer_rsrc_t rs_o = __builtin_amdgcn_make_buffer_rsrc(ws + WS_O + (size_t)(4 * tb3 + kq) * 32768, 0, 32768, 0x00020000);
    const int oo = (g4 >> 1) * 2048 + ((g4 & 1) * 32 + t16) * 16;
    const LAS char* wa = smem + L_WO + ((g4 >> 1) * 32 + (g4 & 1) * 4 + ((lane & 15) >> 2)) * 64 + (lane & 3) * 8;
    const int h0 = (g4 & 1) << 5, h1 = h0 ^ 32;
    await(W_SEL, 1u);
    const unsigned skey = __builtin_amdgcn_readfirstlane(lw[W_SEL]);
    const int sel = 2047 - (int)(skey & 2047u);
    const bool own = (sel >> 7) == tb3;
    u32x4 eb[8];
    auto load_even = [&]() {
#pragma unroll
        for (int j = 0; j < 8; ++j) eb[j] = __builtin_amdgcn_raw_buffer_load_b128(rs_o, oo + hh * 256, j * 4096, OAUXL);
        __builtin_amdgcn_sched_barrier(0); };
    if (hh == 0) {
        publish_o(0);
        __builtin_amdgcn_s_setprio(0);
        if (ffn) {
            const __amdgpu_buffer_rsrc_t rs_p = __builtin_amdgcn_make_buffer_rsrc(ws + WS_P1 + (size_t)(fi >> 3) * 8192, 0, 8192, 0x00020000);
            const int po = (lane >> 3) * 1024 + ((lane >> 2) & 1) * 512 + (fi & 7) * 64 + (lane & 3) * 16;
            u32x4 pv = __builtin_amdgcn_raw_buffer_load_b128(rs_p, po, 0, 16);
            unsigned wd2[8];
#pragma unroll
            for (int f = 0; f < 8; ++f) wd2[f] = *(const unsigned*)(p.wd + (size_t)(8 * fi + f) * 512 + 128 * kq + 2 * lane);
            while (!__all(pv[1] == (unsigned)tag && pv[3] == (unsigned)tag) && spin > 0) {
                --spin; asm volatile("" ::: "memory"); pv = __builtin_amdgcn_raw_buffer_load_b128(rs_p, po, 0, 16); }
            float hv[2];
#pragma unroll
            for (int j = 0; j < 2; ++j) {
                float v = __uint_as_float(pv[2 * j]); v += dppf<0x128>(v);
                auto a = __builtin_amdgcn_permlane16_swap(__float_as_uint(v), __float_as_uint(v), false, false); v = __uint_as_float(a[0]) + __uint_as_float(a[1]);
                auto b = __builtin_amdgcn_permlane32_swap(__float_as_uint(v), __float_as_uint(v), false, false);
                const float gs = bfr(__uint_as_float(b[0]) + __uint_as_float(b[1])), us = dppf<0x124>(gs);
                hv[j] = bfr(bfr(gs * sigmoidf_(gs)) * us); }
            float y0 = 0.f, y1 = 0.f;
#pragma unroll
            for (int f = 0; f < 8; ++f) {
                const float h = __int_as_float(__builtin_amdgcn_readlane(__float_as_int(hv[f & 1]), f >> 1));
                y0 = fmaf(h, bflo(wd2[f]), y0); y1 = fmaf(h, bfhi(wd2[f]), y1); }
            const __amdgpu_buffer_rsrc_t rs_w = __builtin_amdgcn_make_buffer_rsrc(ws + WS_YP + (size_t)fi * 4096, 0, 4096, 0x00020000);
            __builtin_amdgcn_raw_buffer_store_b128((u32x4){__float_as_uint(y0), (unsigned)tag, __float_as_uint(y1), (unsigned)tag}, rs_w, lane * 16, kq * 1024, 16);
        }
        __builtin_amdgcn_s_setprio(2);
        for (;;) {
            unsigned m;
            if (kq == 0) { m = (unsigned)__ballot(__hip_atomic_load((GAS u64*)(ws + WS_OF + (tb3 * 16 + t16) * 128), RLX_AGENT) == tag); lw[W_EM] = m; }
            else m = __builtin_amdgcn_readfirstlane(lw[W_EM]);
            if (m == ~0u || spin <= 0) break;
            --spin; __builtin_amdgcn_s_sleep(8); }
        asm volatile("" ::: "memory");
        load_even();
        while ((unsigned)__builtin_amdgcn_readfirstlane(lw[W_NEAR]) == 0 && spin > 0) { --spin; __builtin_amdgcn_s_sleep(6); }
        asm volatile("" ::: "memory");
        if (own) {
            const __amdgpu_buffer_rsrc_t rs_y = __builtin_amdgcn_make_buffer_rsrc(ws + WS_YP + (size_t)kq * 56 * 4096, 0, 56 * 4096, 0x00020000);
            float s0, s1;
            for (;;) {
                s0 = s1 = 0.f; unsigned bad = 0;
#pragma unroll
                for (int i = 0; i < 14; ++i) {
                    const u32x4 v = __builtin_amdgcn_raw_buffer_load_b128(rs_y, g4 * 4096 + cg3 * 256 + t16 * 16, i * 16384, 16);
                    s0 += __uint_as_float(v[0]); s1 += __uint_as_float(v[2]); bad |= (v[1] ^ (unsigned)tag) | (v[3] ^ (unsigned)tag); }
                if (__all(bad == 0) || spin <= 0) break;
                --spin; __builtin_amdgcn_s_sleep(16); asm volatile("" ::: "memory"); }
            auto a = __builtin_amdgcn_permlane16_swap(__float_as_uint(s0), __float_as_uint(s1), false, false);
            const float v = __uint_as_float(a[0]) + __uint_as_float(a[1]);
            auto b = __builtin_amdgcn_permlane32_swap(__float_as_uint(v), __float_as_uint(v), false, false);
            if (lane < 32) *(LAS float*)(smem + L_YS + kq * 128 + t16 * 8 + g4 * 4) = __uint_as_float(b[0]) + __uint_as_float(b[1]);
            arrive(W_Y);
        }
    } else if ((unsigned)__builtin_amdgcn_readfirstlane(lw[W_EM]) == ~0u) {
        asm volatile("" ::: "memory");
        load_even(); publish_o(8);
    } else {
        publish_o(0); await(W_EM, ~0u); load_even();
    }
    f32x4 e0 = {}, e1 = {};
#pragma unroll
    for (int j = 0; j < 8; ++j) { e0 = mfma16(trd2(wa + j * 4096 + h0, 512), eb[j], e0); e1 = mfma16(trd2(wa + j * 4096 + h1, 512), eb[j], e1); }
    if (own) await(W_Y, 4);
    u32x4 wf[16];
#pragma unroll
    for (int j = 0; j < 8; ++j) { wf[2 * j] = trd2(wa + 1024 + j * 4096 + h0, 512); wf[2 * j + 1] = trd2(wa + 1024 + j * 4096 + h1, 512); }
    while (__any((gg ^ tag) << 16 != 0) && spin > 0) { --spin; gg = __hip_atomic_load(gp, RLX_AGENT); }
    const float gt = bfhi((unsigned)(gg >> 32));
    float bb[8] = {bflo(xa[0]), bfhi(xa[0]), bflo(xa[1]), bfhi(xa[1]), bflo(xc[0]), bfhi(xc[0]), bflo(xc[1]), bfhi(xc[1])};
    if (own && tok == sel) {
        const unsigned mono = (skey >> 11) & 0xffffu, rb = (mono & 0x8000u) ? (mono & 0x7fffu) : (~mono & 0xffffu);
        const float rsel = bfr(sigmoidf_(bflo(rb)));
        f32x4 y0 = {}, y1 = {};
#pragma unroll
        for (int i = 0; i < 4; ++i) { y0 += *(const LAS f32x4*)(smem + L_YS + i * 128 + g4 * 16); y1 += *(const LAS f32x4*)(smem + L_YS + i * 128 + 64 + g4 * 16); }
#pragma unroll
        for (int r = 0; r < 4; ++r) { bb[r] = bfr(bb[r] + bfr(bfr(y0[r]) * rsel)); bb[4 + r] = bfr(bb[4 + r] + bfr(bfr(y1[r]) * rsel)); }
    }
    const float gm = bfr(1.0f - gt);
#pragma unroll
    for (int r = 0; r < 8; ++r) { bb[r] = bfr(gm * bb[r]); asm volatile("" : "+v"(bb[r])); }
#pragma unroll
    for (int j = 0; j < 16; ++j) asm volatile("" : "+v"(wf[j]));
    STAMP(19);
    u32x4 ob[8];
    f32x4 cg[4][2] = {};
    auto loadg = [&](int g) {
#pragma unroll
        for (int j = 2 * g; j < 2 * g + 2; ++j) { ob[j] = __builtin_amdgcn_raw_buffer_load_b128(rs_o, oo + 1024 + hh * 256, j * 4096, OAUXL); __builtin_amdgcn_sched_barrier(0); } };
    auto mulg = [&](int g) {
#pragma unroll
        for (int j = 2 * g; j < 2 * g + 2; ++j) { cg[g][0] = mfma16(wf[2 * j], ob[j], cg[g][0]); cg[g][1] = mfma16(wf[2 * j + 1], ob[j], cg[g][1]); }
        __builtin_amdgcn_sched_barrier(0); };
    const GAS u64* ofp = (const GAS u64*)(ws + WS_OF + ((16 + tb3) * 16 + t16) * 128);
    unsigned todo = 15;
    do {
        unsigned have;
        if (w == 0) { have = (unsigned)__ballot(__hip_atomic_load(ofp, RLX_AGENT) == tag); lw[W_LM] = have; }
        else { have = __builtin_amdgcn_readfirstlane(lw[W_LM]); __builtin_amdgcn_s_sleep(1); }
        if (spin <= 0) have = ~0u;
        --spin; asm volatile("" ::: "memory");
        unsigned go = 0;
#pragma unroll
        for (int g = 0; g < 4; ++g) if ((todo >> g & 1) && ((have >> (4 * g)) & 15u) == 15u) { go |= 1u << g; loadg(g); }
#pragma unroll
        for (int g = 0; g < 4; ++g) if (go >> g & 1) mulg(g);
        todo &= ~go;
    } while (todo);
    STAMP(20);
    const f32x4 c0 = e0 + ((cg[0][0] + cg[1][0]) + (cg[2][0] + cg[3][0])), c1 = e1 + ((cg[0][1] + cg[1][1]) + (cg[2][1] + cg[3][1]));
    f32x4 r0, r1;
#pragma unroll
    for (int r = 0; r < 4; ++r) { r0[r] = bfr(gt * bfr(c0[r])) + bb[r]; r1[r] = bfr(gt * bfr(c1[r])) + bb[4 + r]; }
    { const __amdgpu_buffer_rsrc_t rs_out = __builtin_amdgcn_make_buffer_rsrc(p.out, 0, 2u << 20, 0x00020000);
      __builtin_amdgcn_raw_buffer_store_b128(pack_pair(r0, r1), rs_out, tok * 1024 + 64 * cg3 + 32 * (g4 & 1) + 16 * (g4 >> 1), 0, 0); }
}

extern "C" void kernel_launch(void* const* d_in, const int* in_sizes, int n_in,
                              void* d_out, int out_size, void* d_ws, size_t ws_size,
                              hipStream_t stream) {
    Params p;
    p.x = (const uint16_t*)d_in[0]; p.wqkv = (const uint16_t*)d_in[1]; p.wo = (const uint16_t*)d_in[2];
    p.wr = (const uint16_t*)d_in[4]; p.wg = (const uint16_t*)d_in[5]; p.wu = (const uint16_t*)d_in[6];
    p.wd = (const uint16_t*)d_in[7]; p.gw = (const uint16_t*)d_in[8];
    p.out = (uint16_t*)d_out; p.ws = (char*)d_ws;
    mix_kernel<<<256, 512, 0, stream>>>(p);
}
```

```cpp
#include <hip/hip_runtime.h>
#include <hip/hip_bf16.h>
#include <stdint.h>

typedef __attribute__((ext_vector_type(8))) __bf16 bf16x8;
typedef __attribute__((ext_vector_type(16))) __bf16 bf16x16;
typedef __attribute__((ext_vector_type(4))) float f32x4;
typedef __attribute__((ext_vector_type(8))) float f32x8;
typedef __attribute__((ext_vector_type(16))) float f32x16;
typedef __attribute__((ext_vector_type(4))) unsigned u32x4;
typedef __attribute__((ext_vector_type(2))) unsigned u32x2;
typedef __attribute__((ext_vector_type(4))) short s16x4;
typedef unsigned long long u64;
#define LAS __attribute__((address_space(3)))
#define GAS __attribute__((address_space(1)))
#define RLX_AGENT __ATOMIC_RELAXED, __HIP_MEMORY_SCOPE_AGENT

constexpr size_t WS_KV = 0;
constexpr size_t WS_O = 4u << 20;
constexpr size_t WS_YP = 6u << 20;
constexpr size_t WS_G = 7u << 20;
constexpr size_t WS_RG = WS_G + 16384;
constexpr size_t WS_KVF = WS_RG + 2048;
constexpr size_t WS_OF = WS_KVF + 65536;
constexpr size_t WS_P1 = WS_OF + 65536;
constexpr size_t WS_EP = WS_P1 + 229376;

constexpr int SLOT = 24576, NSLOT = 5;
constexpr int L_WQ = SLOT * NSLOT;
constexpr int L_MISC = L_WQ + 32768;
constexpr int L_RK = L_MISC;
constexpr int L_W = L_MISC + 64;
constexpr int L_YS = L_MISC + 256;
constexpr int L_RW = L_MISC + 2048;
constexpr int LDS_BYTES = L_RW + 2048;
constexpr int L_CB = 0;
constexpr int L_Q = 57344;
constexpr int L_FS = 65536;
constexpr int L_EP = 69632;
constexpr int L_WO = 81920;
enum { W_Q = 0, W_KM = 2, W_C = 4, W_O = 6, W_F = 8, W_Y = 9, W_EM = 10, W_LM = 11, W_SEL = 12, W_PF = 13, W_NEAR = 16, W_GO = 20 };
constexpr unsigned SENT = 0xffffffffu;
constexpr int SPIN_CAP = 200000;
#define POLL 0
#define KVAUX 16
#define OAUXL 16
#define PRIO2 2
#define PFQ 0
#define ROT 4

#define STAMP(i)
#define CSTAMP(i)
#define STAMP_DECL
#define STAMP_OUT

struct Params {
    const uint16_t* x; const uint16_t* wqkv; const uint16_t* wo; const uint16_t* wr;
    const uint16_t* wg; const uint16_t* wu; const uint16_t* wd; const uint16_t* gw;
    uint16_t* out; char* ws;
};

__device__ __forceinline__ float bflo(unsigned u) { return __uint_as_float(u << 16); }
__device__ __forceinline__ float bfhi(unsigned u) { return __uint_as_float(u & 0xffff0000u); }
__device__ __forceinline__ unsigned cvtpk(float lo, float hi) {
    typedef __attribute__((ext_vector_type(2))) float f2; typedef __attribute__((ext_vector_type(2))) __bf16 b2;
    return __builtin_bit_cast(unsigned, __builtin_convertvector((f2){lo, hi}, b2));
}
__device__ __forceinline__ float bfr(float v) { return bflo(cvtpk(v, 0.f) & 0xffffu); }
__device__ __forceinline__ float sigmoidf_(float v) { return __builtin_amdgcn_rcpf(1.0f + __builtin_amdgcn_exp2f(v * -1.4426950408889634f)); }
__device__ __forceinline__ f32x4 mfma16(u32x4 a, u32x4 b, f32x4 c) {
    return __builtin_amdgcn_mfma_f32_16x16x32_bf16(__builtin_bit_cast(bf16x8, a), __builtin_bit_cast(bf16x8, b), c, 0, 0, 0);
}
__device__ __forceinline__ u32x2 trd(const LAS char* p) {
    return __builtin_bit_cast(u32x2, __builtin_amdgcn_ds_read_tr16_b64_v4i16((LAS s16x4*)p));
}
__device__ __forceinline__ u32x4 trd2(const LAS char* p, int d) { u32x2 a = trd(p), b = trd(p + d); return (u32x4){a[0], a[1], b[0], b[1]}; }
__device__ __forceinline__ void dma16(const void* sbase, unsigned voff, unsigned lds) {
    unsigned keep;
    asm volatile("s_mov_b32 %0, m0\n\ts_mov_b32 m0, %3\n\ts_nop 0\n\tglobal_load_lds_dwordx4 %1, %2\n\ts_mov_b32 m0, %0"
                 : "=&s"(keep) : "v"(voff), "s"(sbase), "s"(lds) : "memory");
}
__device__ __forceinline__ void dma16_half(const void* sbase, unsigned voff, unsigned lds) {
    unsigned keep;
    asm volatile("s_mov_b32 %0, m0\n\ts_mov_b32 m0, %3\n\ts_mov_b32 exec_hi, 0\n\tglobal_load_lds_dwordx4 %1, %2\n\ts_mov_b32 exec_hi, -1\n\ts_mov_b32 m0, %0"
                 : "=&s"(keep) : "v"(voff), "s"(sbase), "s"(lds) : "memory");
}
template <int N> __device__ __forceinline__ void wait_bar() {
    asm volatile("" ::: "memory"); __builtin_amdgcn_s_waitcnt(0xc07f);
    asm volatile("s_waitcnt vmcnt(%0) lgkmcnt(0)\n\ts_barrier" :: "n"(N) : "memory"); }
template <int N> __device__ __forceinline__ void vm_bar() { asm volatile("s_waitcnt vmcnt(%0)\n\ts_barrier" :: "n"(N) : "memory"); }
__device__ __forceinline__ void lds_bar() { asm volatile("s_waitcnt lgkmcnt(0)\n\ts_barrier" ::: "memory"); }
__device__ __forceinline__ void bare_bar() { asm volatile("s_barrier" ::: "memory"); }
template <int CTRL> __device__ __forceinline__ float dppf(float v) {
    return __int_as_float(__builtin_amdgcn_update_dpp(0, __float_as_int(v), CTRL, 0xf, 0xf, true));
}
template <int CTRL> __device__ __forceinline__ unsigned dppu(unsigned v) {
    return (unsigned)__builtin_amdgcn_update_dpp(0, (int)v, CTRL, 0xf, 0xf, true);
}
__device__ __forceinline__ float row_sum16(float v) {
    v += dppf<0x128>(v); v += dppf<0x124>(v); v += dppf<0x122>(v); v += dppf<0x121>(v); return v;
}
__device__ __forceinline__ float wave_sum(float v) {
    v = row_sum16(v);
    auto a = __builtin_amdgcn_permlane16_swap(__float_as_uint(v), __float_as_uint(v), false, false);
    v = __uint_as_float(a[0]) + __uint_as_float(a[1]);
    auto b = __builtin_amdgcn_permlane32_swap(__float_as_uint(v), __float_as_uint(v), false, false);
    return __uint_as_float(b[0]) + __uint_as_float(b[1]);
}
__device__ __forceinline__ unsigned wave_maxu(unsigned v) {
    v = max(v, dppu<0x128>(v)); v = max(v, dppu<0x124>(v)); v = max(v, dppu<0x122>(v)); v = max(v, dppu<0x121>(v));
    auto a = __builtin_amdgcn_permlane16_swap(v, v, false, false); v = max(a[0], a[1]);
    auto b = __builtin_amdgcn_permlane32_swap(v, v, false, false); return max(b[0], b[1]);
}
__device__ __forceinline__ u32x4 pack_pair(f32x4 a, f32x4 b) {
    auto x = __builtin_amdgcn_permlane16_swap(cvtpk(a[0], a[1]), cvtpk(b[0], b[1]), false, false);
    auto y = __builtin_amdgcn_permlane16_swap(cvtpk(a[2], a[3]), cvtpk(b[2], b[3]), false, false);
    return (u32x4){x[0], y[0], x[1], y[1]};
}
#define STR2(x) #x
#define STR(x) STR2(x)
#define EX(r) "v_exp_f32 v" STR(r) ", v" STR(r) "\n\t"
#define QKM(S, K, Q) "v_mfma_f32_32x32x16_bf16 " S ", " K ", " Q ", 0\n\t"
#define PVM(O, V, P) "v_smfmac_f32_32x32x32_bf16 " O ", " V ", " P ", %[idx]\n\t"
#define UNIT_A(O, V0, V1, MID) EX(194) EX(195) EX(196) MID EX(197) EX(198) EX(199) EX(200) EX(201) PVM(O, V0, "v[192:199]") \
    EX(202) EX(203) EX(204) EX(205) EX(206) EX(207) EX(208) EX(209) PVM(O, V1, "v[200:207]")
#define UNIT_B(O, V0, V1, MID) EX(210) EX(211) EX(212) MID EX(213) EX(214) EX(215) EX(216) EX(217) PVM(O, V0, "v[208:215]") \
    EX(218) EX(219) EX(220) EX(221) EX(222) EX(223) EX(192) EX(193) PVM(O, V1, "v[216:223]")
#define UNIT_B_LAST(O, V0, V1) EX(210) EX(211) EX(212) EX(213) EX(214) EX(215) EX(216) EX(217) PVM(O, V0, "v[208:215]") \
    EX(218) EX(219) EX(220) EX(221) EX(222) EX(223) "s_nop 1\n\t" PVM(O, V1, "v[216:223]")
#define SA "v[192:207]"
#define SB "v[208:223]"
#define S_CLOB "v192", "v193", "v194", "v195", "v196", "v197", "v198", "v199", "v200", "v201", "v202", "v203", "v204", "v205", "v206", "v207", \
    "v208", "v209", "v210", "v211", "v212", "v213", "v214", "v215", "v216", "v217", "v218", "v219", "v220", "v221", "v222", "v223"
#define LOADT(J, K, V0, V1) "s_xor_b32 %[t], %[sb], " J "\n\t" \
    "buffer_load_dwordx4 " K ", %[ko], %[rs], %[t] offen sc1\n\t" \
    "s_mov_b64 exec, %[vm]\n\t" \
    "buffer_load_dwordx4 " V0 ", %[ko], %[rs], %[t] offen offset:1024 sc1\n\t" \
    "buffer_load_dwordx4 " V1 ", %[ko], %[rs], %[t] offen offset:1280 sc1\n\t" \
    "s_mov_b64 exec, -1\n\t"
#define GROUP "s_add_u32 %[gi], %[gi], 0x8000\n\ts_and_b32 %[gi], %[gi], 0x18000\n\ts_xor_b32 %[sb], %[so], %[gi]\n\t" \
    "s_lshr_b32 %[t], %[sb], 13\n\ts_and_b32 %[t], %[t], 12\n\ts_lshl_b32 %[t], 15, %[t]\n\t" \
    "s_andn2_b32 %[u], %[t], %[rdy]\n\ts_cbranch_scc0 3f\n\t" \
    "2:\n\ts_waitcnt vmcnt(0)\n\t" \
    "v_cmp_eq_u64 vcc, %[tag], %[fl]\n\ts_nop 4\n\ts_and_b32 %[rdy], vcc_lo, 0xffff\n\t" \
    "s_andn2_b32 %[u], %[t], %[rdy]\n\ts_cbranch_scc0 3f\n\t" \
    "global_load_dwordx2 %[fl], %[fo], %[fb] sc1\n\t" \
    "s_sub_i32 %[spin], %[spin], 1\n\ts_cmp_gt_i32 %[spin], 0\n\ts_cbranch_scc1 2b\n\t" \
    "3:\n\t"
#define TILE(B, BN, LD) \
    UNIT_A("%[o0]", "%[" B "v0]", "%[" B "v1]", QKM(SB, "%[" B "k]", "%[q1]") LD) \
    UNIT_B("%[o1]", "%[" B "v0]", "%[" B "v1]", QKM(SA, "%[" B "k]", "%[q2]")) \
    UNIT_A("%[o2]", "%[" B "v0]", "%[" B "v1]", QKM(SB, "%[" B "k]", "%[q3]")) \
    UNIT_B("%[o3]", "%[" B "v0]", "%[" B "v1]", "s_waitcnt vmcnt(6)\n\t" QKM(SA, "%[" BN "k]", "%[q0]"))
#define WODMA "s_cmp_lg_u32 %[hh], 0\n\ts_cbranch_scc1 5f\n\ts_mov_b32 m0, %[wl]\n\ts_add_u32 %[wl], %[wl], 0x400\n\t" \
    "global_load_lds_dwordx4 %[wv], %[wp]\n\tv_add_u32 %[wv], 0x4000, %[wv]\n\t5:\n\t"
#define FPRE "s_cmp_lg_u32 %[hh], 0\n\ts_cbranch_scc1 5f\n\tglobal_load_dword %[fd], %[fa], off\n\t5:\n\t"
#define LOADB(J, B) LOADT(J, "%[" B "k]", "%[" B "v0]", "%[" B "v1]")

__global__ __launch_bounds__(512, 2) void mix_kernel(Params p) {
    __shared__ __attribute__((aligned(16))) char smem_[LDS_BYTES];
    LAS char* smem = (LAS char*)smem_;
    const unsigned lds0 = (unsigned)(uintptr_t)smem;
    const int tid = threadIdx.x, lane = tid & 63;
    const int w = __builtin_amdgcn_readfirstlane(tid >> 6);
    const int xcd = blockIdx.x & 7, jj = blockIdx.x >> 3;
    const int tb = 4 * (xcd >> 1) + (jj & 3), hp = 8 * (xcd & 1) + (jj >> 2);
    const int kq = w & 3, hh = w >> 2, head = 2 * hp + hh;
    const int g4 = lane >> 4, t16 = lane & 15;
    char* const ws = p.ws;
    const __amdgpu_buffer_rsrc_t rs_kv = __builtin_amdgcn_make_buffer_rsrc(ws + WS_KV + (size_t)head * 131072 + kq * 2048, 0, 131072 - kq * 2048, 0x00020000);
    int spin = SPIN_CAP;
    STAMP_DECL; STAMP(0);
    asm volatile("" :: "s"(p.x), "s"(p.wqkv), "s"(p.wo), "s"(p.wr), "s"(p.wg), "s"(p.wu), "s"(p.wd), "s"(p.gw), "s"(p.out), "s"(p.ws));
    STAMP(24);

    const int tokr = 128 * tb + 8 * hp + w;
    u64 ep;
    asm volatile("s_load_dwordx2 %0, %1, 0x0" : "=s"(ep) : "s"(ws + WS_EP) : "memory");
    if (w < 2) dma16(w ? p.gw : p.wr, lane * 16, lds0 + L_RW + w * 1024);
    unsigned pq = 0;
    if (PFQ && w == 7) asm volatile("global_load_dword %0, %1, %2" : "=v"(pq) : "v"((jj * 16 + (lane >> 2)) * 3072 + (xcd & 1) * 512 + (lane & 3) * 128), "s"(p.wqkv) : "memory");
    u32x4 rx;
    asm volatile("global_load_dwordx4 %0, %1, %2" : "=v"(rx) : "v"(lane * 16), "s"(p.x + (size_t)tokr * 512) : "memory");
    unsigned xo0, xo1, wo0;
    { const int t0 = 16 * w + (lane >> 3), t1 = t0 + 8;
      xo0 = t0 * 1024 + (((lane & 7) ^ ((t0 >> 1) & 7)) << 4);
      xo1 = t1 * 1024 + (((lane & 7) ^ ((t1 >> 1) & 7)) << 4);
      const int k0 = 16 * (w & 3) + (lane >> 2), pc = ((lane & 3) ^ (((k0 >> 3) & 1) << 1)) << 4;
      wo0 = k0 * 3072 + 1024 + (w >> 2) * 1024 + pc; }
    const char* xb = (const char*)p.x + (size_t)tb * 131072;
    const char* wb = (const char*)p.wqkv + hp * 64;
    const int rot = ROT == 0 ? 0 : ROT == 1 ? (2 * (jj & 3) + (jj >> 2)) & 7 : ROT == 2 ? (((jj & 3) + (jj >> 2)) & 1) * 4 : ((jj & 3) + (jj >> 2) * 2 + (jj >> 3)) & 3;
    const int rx4 = ROT == 5 ? rot ^ (((xcd ^ (xcd >> 1)) & 1) << 2) : rot;
    auto chunk = [&](int c) { return ROT >= 4 ? c ^ rx4 : (c + rot) & 7; };
    auto issue = [&](int c, int i) {
        const unsigned s = lds0 + (c % NSLOT) * SLOT; const int cc = chunk(c);
        if (i == 0) dma16(xb + cc * 128, xo0, s + w * 2048);
        if (i == 1) dma16(wb + cc * 196608, wo0, s + 16384 + w * 1024);
        if (i == 2) dma16(xb + cc * 128, xo1, s + w * 2048 + 1024);
    };
    const __amdgpu_buffer_rsrc_t rs_os = __builtin_amdgcn_make_buffer_rsrc(ws + WS_O + (size_t)(4 * tb + kq) * 32768 + head * 1024, 0, 1024, 0x00020000);
#pragma unroll
    for (int i = 0; i < 3; ++i) issue(0, i);
    if (tid < 32) *(LAS unsigned*)(smem + L_W + 4 * tid) = 0u;
    asm volatile("s_waitcnt vmcnt(3) lgkmcnt(0)\n\ts_barrier" : "+v"(rx), "+s"(ep) : "v"(pq) : "memory");
    const u64 tag = ep + 1;
    unsigned gbits;
    {
            const u32x4 rw = *(const LAS u32x4*)(smem + L_RW + lane * 16), rgw = *(const LAS u32x4*)(smem + L_RW + 1024 + lane * 16);
            float ra = 0.f, ga = 0.f;
#pragma unroll
            for (int j = 0; j < 4; ++j) {
                const float a = bflo(rx[j]), b = bfhi(rx[j]);
                ra = fmaf(a, bflo(rw[j]), ra); ra = fmaf(b, bfhi(rw[j]), ra);
                const unsigned pr = cvtpk(a * bflo(rgw[j]), b * bfhi(rgw[j]));
                ga += bflo(pr); ga += bfhi(pr); }
            ra = wave_sum(ra); ga = wave_sum(ga);
            const unsigned rb = cvtpk(ra, 0.f) & 0xffffu;
            const unsigned key = (((rb & 0x8000u) ? (~rb & 0xffffu) : (rb | 0x8000u)) << 11) | (unsigned)(2047 - tokr);
            gbits = cvtpk(sigmoidf_(bfr(ga)), 0.f) & 0xffffu;
            if (lane == 0) *(LAS unsigned*)(smem + L_RK + 4 * w) = key; }
#pragma unroll
    for (int c = 1; c < 3; ++c) {
#pragma unroll
        for (int i = 0; i < 3; ++i) issue(c, i);
        if (c == 1) { lds_bar();
            if (w == 0) {
                const unsigned kk = wave_maxu(*(const LAS unsigned*)(smem + L_RK + 4 * (lane & 7)));
                if (lane == 0) __hip_atomic_store((GAS u64*)(ws + WS_RG) + blockIdx.x, ((u64)kk << 37) | (tag & ((1ull << 37) - 1)), RLX_AGENT);
            }
        }
    }
    const int fi = min((int)blockIdx.x, 223); const bool ffn = blockIdx.x < 224;
    const __amdgpu_buffer_rsrc_t rs_rg = __builtin_amdgcn_make_buffer_rsrc(ws + WS_RG, 0, 2048, 0x00020000);
    STAMP(1);
    u32x4 kf, vc, qf;
    const int frow = 64 * (fi & 7) + 16 * kq + (lane >> 3);
    {   f32x4 aq[2] = {}, ak[2] = {}, av[2] = {};
        const int fsw = (t16 >> 1) & 7;
        const int xa = (32 * kq + t16) * 128 + ((g4 ^ fsw) << 4);
        const int wa = g4 * 512 + ((lane & 15) >> 2) * 64 + ((hh ^ (g4 & 1)) << 5) + (lane & 3) * 8;
        u32x4 fx[8][4], fw[2][4];
        auto frag = [&](int c, int i) {
            const LAS char* s = smem + (c % NSLOT) * SLOT; const int kk = i >> 2, j = i & 3;
            if (j == 1 || j == 2) fx[c][2 * kk + j - 1] = *(const LAS u32x4*)(s + (xa ^ (kk * 64)) + (j - 1) * 2048);
            else fw[c & 1][2 * kk + (j ? 1 : 0)] = trd2(s + 16384 + wa + (j ? 4096 : 0) + kk * 2048, 256); };
        wait_bar<6>();
        STAMP(3); CSTAMP(24);
#pragma unroll
        for (int i = 0; i < 8; ++i) { frag(0, i); if (i < 3) issue(3, i); __builtin_amdgcn_sched_barrier(0); }
#pragma unroll
        for (int c = 0; c < 8; ++c) {
            if (c < 5) vm_bar<6>(); else if (c == 5) vm_bar<5>(); else if (c == 6) vm_bar<4>();
            if (c == 1) { STAMP(6); CSTAMP(25); }
            if (c == 2) { STAMP(11); CSTAMP(26); }
            if (c == 3) { STAMP(7); CSTAMP(27); }
            if (c == 5) { STAMP(9); CSTAMP(28); }
            if (c == 6) { STAMP(10); }
            __builtin_amdgcn_sched_barrier(0);
#pragma unroll
            for (int i = 0; i < 8; ++i) {
                const int kk = i >> 2, j = i & 3; const u32x4 xx = fx[c][2 * kk + (j & 1)], ww = fw[c & 1][2 * kk + (j >> 1)];
                if (j < 2) ak[j] = mfma16(ww, xx, ak[j]); else av[j - 2] = mfma16(xx, ww, av[j - 2]);
                if (c < 7 && i < 4) { frag(c + 1, 2 * i); frag(c + 1, 2 * i + 1); }
                if (c < 4 && (i & 1) == 0 && i < 6) issue(c + 4, i >> 1);
                if ((c == 4 || c == 5) && (i & 3) == 1) { const int n = (2 * (c - 4) + (i >> 2)) ^ (PFQ ? 0 : rot & 3);
                    dma16(wb + (64 * w + 16 * n) * 3072, (lane >> 2) * 3072 + (((lane & 3) ^ ((lane >> 4) & 2)) << 4), lds0 + L_WQ + (4 * w + n) * 1024); }
                __builtin_amdgcn_sched_barrier(0);
            }
        }
        kf = pack_pair(ak[0], ak[1]); vc = pack_pair(av[0], av[1]);
        asm volatile("s_waitcnt vmcnt(0)\n\ts_barrier" : "+v"(kf), "+v"(vc) :: "memory");
        __builtin_amdgcn_raw_buffer_store_b128(kf, rs_kv, lane * 16, tb * 8192, 16);
        __builtin_amdgcn_raw_buffer_store_b128(vc, rs_kv, lane * 16 + 1024, tb * 8192, 16);
        STAMP(12); CSTAMP(29);
        u32x4 fq[8][2];
        auto fragq = [&](int c) { const LAS char* s = smem + L_WQ + wa + chunk(c) * 4096; fq[c][0] = trd2(s, 256); fq[c][1] = trd2(s + 2048, 256); };
        fragq(0); fragq(1); fragq(2);
        __builtin_amdgcn_sched_barrier(0);
#pragma unroll
        for (int c = 0; c < 8; ++c) {
            aq[0] = mfma16(fq[c][0], fx[c][0], aq[0]); aq[1] = mfma16(fq[c][0], fx[c][1], aq[1]);
            if (c + 3 < 8) fragq(c + 3);
            __builtin_amdgcn_sched_barrier(0);
            aq[0] = mfma16(fq[c][1], fx[c][2], aq[0]); aq[1] = mfma16(fq[c][1], fx[c][3], aq[1]);
            __builtin_amdgcn_sched_barrier(0);
        }
        constexpr float QS = 0.25f * 1.4426950408889634f;
        qf = pack_pair(aq[0] * QS, aq[1] * QS);
        *(LAS u32x4*)(smem + L_Q + w * 1024 + lane * 16) = qf;
        __builtin_amdgcn_sched_barrier(0);
    }
    STAMP(4);

    volatile LAS unsigned* lw = (volatile LAS unsigned*)(smem + L_W);
    auto arrive = [&](int word) -> unsigned {
        unsigned t = 0; asm volatile("s_waitcnt lgkmcnt(0)" ::: "memory");
        if (lane == 0) t = __hip_atomic_fetch_add((LAS unsigned*)(smem + L_W) + word, 1u, __ATOMIC_RELAXED, __HIP_MEMORY_SCOPE_WORKGROUP);
        return __builtin_amdgcn_readfirstlane(t); };
    auto await = [&](int word, unsigned n) {
        while ((unsigned)__builtin_amdgcn_readfirstlane(lw[word]) < n && spin > 0) { --spin; __builtin_amdgcn_s_sleep(2); }
        asm volatile("" ::: "memory"); };
    const unsigned pat = ((lane & 11) == 0) ? 0x3f803f80u : 0u;
    u32x4 xv0, xv1;
#pragma unroll
    for (int j = 0; j < 4; ++j) { auto r = __builtin_amdgcn_permlane16_swap(vc[j], pat, false, false); xv0[j] = r[0]; xv1[j] = r[1]; }
    f32x16 o0 = {}, o1 = {}, o2 = {}, o3 = {};
    const int idx = 0xdddd;
    const char* kvf = ws + WS_KVF + head * 2048;
    asm volatile("" : "+v"(o0), "+v"(o1), "+v"(o2), "+v"(o3), "+v"(xv0), "+v"(xv1));
    __builtin_amdgcn_s_waitcnt(0x0070);
    asm volatile("s_waitcnt vmcnt(0)" ::: "memory");
    if (arrive(W_Q + hh) == 3 && lane == 0) asm volatile("global_store_dwordx2 %0, %1, %2 sc1" :: "v"(tb * 128), "v"(tag), "s"(kvf) : "memory");
    if (hh == 1) __builtin_amdgcn_raw_buffer_store_b128((u32x4){SENT, SENT, SENT, SENT}, rs_os, lane * 16, 0, 16);
    u32x4 rg0, rg1;
    if (w == 4) { rg0 = __builtin_amdgcn_raw_buffer_load_b128(rs_rg, lane * 32, 0, 16); rg1 = __builtin_amdgcn_raw_buffer_load_b128(rs_rg, lane * 32 + 16, 0, 16); }
    await(W_Q + hh, 4);
    STAMP(13);
    const u32x4 q0 = qf;
    const u32x4 q1 = *(const LAS u32x4*)(smem + L_Q + (hh * 4 + ((kq + 1) & 3)) * 1024 + lane * 16);
    const u32x4 q2 = *(const LAS u32x4*)(smem + L_Q + (hh * 4 + ((kq + 2) & 3)) * 1024 + lane * 16);
    const u32x4 q3 = *(const LAS u32x4*)(smem + L_Q + (hh * 4 + ((kq + 3) & 3)) * 1024 + lane * 16);
    asm volatile(
        "s_nop 1\n\t" QKM(SA, "%[k]", "%[q0]") "s_nop 11\n\t" EX(192) EX(193)
        UNIT_A("%[o0]", "%[v0]", "%[v1]", QKM(SB, "%[k]", "%[q1]"))
        UNIT_B("%[o1]", "%[v0]", "%[v1]", QKM(SA, "%[k]", "%[q2]"))
        UNIT_A("%[o2]", "%[v0]", "%[v1]", QKM(SB, "%[k]", "%[q3]"))
        UNIT_B_LAST("%[o3]", "%[v0]", "%[v1]") "s_nop 11"
        : [o0] "+v"(o0), [o1] "+v"(o1), [o2] "+v"(o2), [o3] "+v"(o3)
        : [k] "v"(kf), [v0] "v"(xv0), [v1] "v"(xv1), [q0] "v"(q0), [q1] "v"(q1), [q2] "v"(q2), [q3] "v"(q3), [idx] "v"(idx)
        : S_CLOB);
    STAMP(15);
    __builtin_amdgcn_s_setprio(2);
    if (w == 4) {
        const unsigned tl = (unsigned)tag, th = (unsigned)(tag >> 32) & 31u;
        auto fresh = [&]() { return __all((((rg0[0] ^ tl) | (rg0[2] ^ tl) | (rg1[0] ^ tl) | (rg1[2] ^ tl)) | (((rg0[1] ^ th) | (rg0[3] ^ th) | (rg1[1] ^ th) | (rg1[3] ^ th)) & 31u)) == 0); };
        if (!fresh()) do {
            --spin; asm volatile("" ::: "memory");
            rg0 = __builtin_amdgcn_raw_buffer_load_b128(rs_rg, lane * 32, 0, 16); rg1 = __builtin_amdgcn_raw_buffer_load_b128(rs_rg, lane * 32 + 16, 0, 16);
        } while (!fresh() && spin > 0);
        const unsigned kk = __builtin_amdgcn_readfirstlane(wave_maxu(max(max(rg0[1], rg0[3]), max(rg1[1], rg1[3])) >> 5));
        lw[W_SEL] = kk | 0x80000000u;
        if (blockIdx.x == 0 && lane == 0) __hip_atomic_store((GAS u64*)(ws + WS_EP), tag, RLX_AGENT);
    }
    { const unsigned need = (15u << (tb & 12)) & ~(1u << tb);
      for (;;) {
          unsigned m;
          if (kq == 0) { m = (unsigned)__ballot(__hip_atomic_load((GAS u64*)(kvf + t16 * 128), RLX_AGENT) == tag) & 0xffffu; lw[W_KM + hh] = m; }
          else { m = __builtin_amdgcn_readfirstlane(lw[W_KM + hh]); __builtin_amdgcn_s_sleep(1); }
          if ((m & need) == need || spin <= 0) break;
          --spin; }
      asm volatile("" ::: "memory"); }
    STAMP(2);
    u32x4 yk, yv0, yv1, zk, zv0, zv1, uk, uv0, uv1;
    yv0 = yv1 = zv0 = zv1 = uv0 = uv1 = (u32x4){pat, pat, pat, pat};
    { const int s1 = (tb ^ 1) * 8192, s2 = (tb ^ 2) * 8192, s3 = (tb ^ 3) * 8192;
      yk = __builtin_amdgcn_raw_buffer_load_b128(rs_kv, lane * 16, s1, KVAUX);
      if ((lane & 16) == 0) {
          yv0 = __builtin_amdgcn_raw_buffer_load_b128(rs_kv, lane * 16 + 1024, s1, KVAUX); yv1 = __builtin_amdgcn_raw_buffer_load_b128(rs_kv, lane * 16 + 1280, s1, KVAUX);
          zv0 = __builtin_amdgcn_raw_buffer_load_b128(rs_kv, lane * 16 + 1024, s2, KVAUX); zv1 = __builtin_amdgcn_raw_buffer_load_b128(rs_kv, lane * 16 + 1280, s2, KVAUX);
          uv0 = __builtin_amdgcn_raw_buffer_load_b128(rs_kv, lane * 16 + 1024, s3, KVAUX); uv1 = __builtin_amdgcn_raw_buffer_load_b128(rs_kv, lane * 16 + 1280, s3, KVAUX); }
      zk = __builtin_amdgcn_raw_buffer_load_b128(rs_kv, lane * 16, s2, KVAUX);
      uk = __builtin_amdgcn_raw_buffer_load_b128(rs_kv, lane * 16, s3, KVAUX); }
    u64 fl = __hip_atomic_load((GAS u64*)(kvf + t16 * 128), RLX_AGENT);
    if (hh == 1) {
        u32x4 wv4[4];
#pragma unroll
        for (int j = 0; j < 4; ++j) wv4[j] = *(const u32x4*)(((j & 1) ? p.wu : p.wg) + (size_t)(frow + 8 * (j >> 1)) * 1792 + 64 * (fi >> 3) + 8 * (lane & 7));
        await(W_SEL, 1u);
        const uint16_t* xr = p.x + (size_t)(2047 - (int)(__builtin_amdgcn_readfirstlane(lw[W_SEL]) & 2047u)) * 512 + frow;
        const float xf0 = bflo(xr[0]), xf1 = bflo(xr[8]);
        float a[16];
#pragma unroll
        for (int j = 0; j < 4; ++j) {
            a[2 * j] = fmaf(xf1, bflo(wv4[2][j]), xf0 * bflo(wv4[0][j])); a[2 * j + 1] = fmaf(xf1, bfhi(wv4[2][j]), xf0 * bfhi(wv4[0][j]));
            a[8 + 2 * j] = fmaf(xf1, bflo(wv4[3][j]), xf0 * bflo(wv4[1][j])); a[9 + 2 * j] = fmaf(xf1, bfhi(wv4[3][j]), xf0 * bfhi(wv4[1][j])); }
#pragma unroll
        for (int j = 0; j < 8; ++j) { auto r = __builtin_amdgcn_permlane32_swap(__float_as_uint(a[j]), __float_as_uint(a[j + 8]), false, false); a[j] = __uint_as_float(r[0]) + __uint_as_float(r[1]); }
#pragma unroll
        for (int j = 0; j < 4; ++j) { auto r = __builtin_amdgcn_permlane16_swap(__float_as_uint(a[j]), __float_as_uint(a[j + 4]), false, false); a[j] = __uint_as_float(r[0]) + __uint_as_float(r[1]); }
        const f32x4 sm = {a[0] + dppf<0x128>(a[0]), a[1] + dppf<0x128>(a[1]), a[2] + dppf<0x128>(a[2]), a[3] + dppf<0x128>(a[3])};
        if ((lane & 8) == 0) *(LAS f32x4*)(smem + L_FS + kq * 512 + (lane >> 5) * 256 + (lane & 7) * 32 + (g4 & 1) * 16) = sm;
        arrive(W_F);
        if (ffn && kq < 2) {
            await(W_F, 4);
            float ps = 0.f;
#pragma unroll
            for (int i = 0; i < 4; ++i) ps += *(const LAS float*)(smem + L_FS + i * 512 + (kq * 64 + lane) * 4);
            __hip_atomic_store((GAS u64*)(ws + WS_P1) + fi * 128 + kq * 64 + lane, ((u64)(unsigned)tag << 32) | __float_as_uint(ps), RLX_AGENT); }
    }
    STAMP(8);
    asm volatile("" :: "v"(yk), "v"(yv0), "v"(yv1), "v"(zk), "v"(zv0), "v"(zv1), "v"(uk), "v"(uv0), "v"(uv1), "v"(fl) : "memory");
    if (hh == 1) { asm volatile("s_waitcnt vmcnt(0)" ::: "memory"); arrive(W_PF); }
    if (lane == 0) __hip_atomic_store((GAS u64*)(ws + WS_G) + tokr, ((u64)gbits << 48) | (tag & ((1ull << 48) - 1)), RLX_AGENT);
    const int tb3 = 2 * xcd + (jj & 1), cg3 = jj >> 1;
    STAMP(16);
    CSTAMP(30);
    { int gi = 0, cnt = 4, sb, t, u;
      const u64 vm = 0x0000ffff0000ffffull;
      int rdy = (int)(unsigned)__ballot(fl == tag) & 0xffff, m0s;
      unsigned wl = lds0 + L_WO + kq * 8192, wv = (128 * kq + (lane >> 2)) * 1024 + (((lane & 3) ^ ((lane >> 3) & 2)) << 4);
      asm volatile(
          "s_mov_b32 %[m0s], m0\n\ts_setprio 1\n\t" QKM(SA, "%[yk]", "%[q0]") "s_nop 11\n\t" EX(192) EX(193)
          "1:\n\t"
          TILE("y", "z", GROUP LOADB("0", "x"))
          TILE("z", "u", WODMA LOADB("0x2000", "y"))
          TILE("u", "x", WODMA LOADB("0x4000", "z"))
          "s_sub_u32 %[cnt], %[cnt], 1\n\ts_cmp_eq_u32 %[cnt], 0\n\ts_cbranch_scc1 4f\n\t"
          TILE("x", "y", LOADB("0x6000", "u"))
          "s_branch 1b\n\t"
          "4:\n\ts_mov_b32 m0, %[m0s]\n\ts_setprio 2\n\ts_waitcnt vmcnt(0)\n\ts_nop 11"
          : [o0] "+v"(o0), [o1] "+v"(o1), [o2] "+v"(o2), [o3] "+v"(o3),
            [xk] "+v"(kf), [xv0] "+v"(xv0), [xv1] "+v"(xv1), [yk] "+v"(yk), [yv0] "+v"(yv0), [yv1] "+v"(yv1),
            [zk] "+v"(zk), [zv0] "+v"(zv0), [zv1] "+v"(zv1), [uk] "+v"(uk), [uv0] "+v"(uv0), [uv1] "+v"(uv1),
            [gi] "+s"(gi), [cnt] "+s"(cnt), [rdy] "+s"(rdy), [spin] "+s"(spin),
            [sb] "=&s"(sb), [t] "=&s"(t), [u] "=&s"(u), [fl] "+v"(fl), [m0s] "=&s"(m0s), [wl] "+s"(wl), [wv] "+v"(wv)
          : [q0] "v"(q0), [q1] "v"(q1), [q2] "v"(q2), [q3] "v"(q3), [idx] "v"(idx), [ko] "v"(lane * 16),
            [rs] "s"(rs_kv), [vm] "s"(vm), [fo] "v"(t16 * 128), [fb] "s"(kvf), [tag] "s"(tag), [so] "s"(tb * 8192),
            [wp] "s"((const char*)p.wo + cg3 * 64), [hh] "s"(hh)
          : S_CLOB, "vcc", "scc", "memory"); }
    __builtin_amdgcn_s_waitcnt(0x0f70);
    STAMP(17);
    unsigned wd2[8];
    if (hh == 1) { __builtin_amdgcn_s_setprio(3); lw[W_NEAR + kq] = 1u; }
    else {
#pragma unroll
        for (int f = 0; f < 8; ++f) wd2[f] = *(const unsigned*)(p.wd + (size_t)(8 * fi + f) * 512 + 128 * kq + 2 * lane);
    }
    CSTAMP(31);
    const int tok = 128 * tb3 + 32 * kq + 16 * hh + t16;
    const GAS u64* gp = (const GAS u64*)(ws + WS_G) + tok;
    u64 gg = __hip_atomic_load(gp, RLX_AGENT);
    const u32x2 xa = *(const u32x2*)(p.x + (size_t)tok * 512 + 32 * cg3 + 4 * g4), xc = *(const u32x2*)(p.x + (size_t)tok * 512 + 32 * cg3 + 16 + 4 * g4);
    { auto put = [&](const f32x16& o, int j) {
          LAS char* d = smem + L_CB + ((hh * 4 + ((kq + j) & 3)) * 3 + (j - 1)) * 2304;
          *(LAS f32x4*)(d + lane * 16) = (f32x4){o[0], o[1], o[2], o[3]};
          *(LAS f32x4*)(d + 1024 + lane * 16) = (f32x4){o[4], o[5], o[6], o[7]};
          *(LAS float*)(d + 2048 + lane * 4) = o[8]; };
      put(o1, 1); put(o2, 2); put(o3, 3); }
    arrive(W_C + hh); await(W_C + hh, 4);
    STAMP(18);
    while (__any((gg ^ tag) << 16 != 0) && spin > 0) { --spin; gg = __hip_atomic_load(gp, RLX_AGENT); }
    asm volatile("" :: "v"(xa), "v"(xc));
    float o[9];
#pragma unroll
    for (int r = 0; r < 9; ++r) o[r] = o0[r];
#pragma unroll
    for (int j = 0; j < 3; ++j) {
        const LAS char* d = smem + L_CB + (w * 3 + j) * 2304;
        const f32x4 a = *(const LAS f32x4*)(d + lane * 16), b = *(const LAS f32x4*)(d + 1024 + lane * 16);
#pragma unroll
        for (int r = 0; r < 4; ++r) { o[r] += a[r]; o[4 + r] += b[r]; }
        o[8] += *(const LAS float*)(d + 2048 + lane * 4);
    }
    { float mag = 0.f;
#pragma unroll
      for (int r = 0; r < 8; ++r) mag += fabsf(o[r]);
      const bool safe = o[8] >= 9.313225746154785e-10f && o[8] <= 1.8446744073709552e19f && mag <= 3.0e38f;
      if (!__all(safe)) {
          f32x16 acc = {}; float m = -INFINITY;
          u32x4 v0 = {pat, pat, pat, pat}, v1 = v0;
          const __amdgpu_buffer_rsrc_t rs_all = __builtin_amdgcn_make_buffer_rsrc(ws + WS_KV + (size_t)head * 131072, 0, 131072, 0x00020000);
          for (int t = 0; t < 64; ++t) {
              const u32x4 kk = __builtin_amdgcn_raw_buffer_load_b128(rs_all, lane * 16, t * 2048, 16);
              if ((lane & 16) == 0) { v0 = __builtin_amdgcn_raw_buffer_load_b128(rs_all, lane * 16 + 1024, t * 2048, 16); v1 = __builtin_amdgcn_raw_buffer_load_b128(rs_all, lane * 16 + 1280, t * 2048, 16); }
              f32x16 s = __builtin_amdgcn_mfma_f32_32x32x16_bf16(__builtin_bit_cast(bf16x8, kk), __builtin_bit_cast(bf16x8, q0), (f32x16){}, 0, 0, 0);
              float tm = s[0];
#pragma unroll
              for (int r = 1; r < 16; ++r) tm = fmaxf(tm, s[r]);
              auto sw = __builtin_amdgcn_permlane32_swap(__float_as_uint(tm), __float_as_uint(tm), false, false);
              const float mn = fmaxf(m, fmaxf(__uint_as_float(sw[0]), __uint_as_float(sw[1])));
              const float al = __builtin_amdgcn_exp2f(m - mn); m = mn;
#pragma unroll
              for (int r = 0; r < 16; ++r) { s[r] = __builtin_amdgcn_exp2f(s[r] - mn); acc[r] *= al; }
              acc = __builtin_amdgcn_smfmac_f32_32x32x32_bf16(__builtin_bit_cast(bf16x8, v0), __builtin_bit_cast(bf16x16, __builtin_shufflevector(s, s, 0, 1, 2, 3, 4, 5, 6, 7)), acc, idx, 0, 0);
              acc = __builtin_amdgcn_smfmac_f32_32x32x32_bf16(__builtin_bit_cast(bf16x8, v1), __builtin_bit_cast(bf16x16, __builtin_shufflevector(s, s, 8, 9, 10, 11, 12, 13, 14, 15)), acc, idx, 0, 0);
          }
#pragma unroll
          for (int r = 0; r < 9; ++r) o[r] = acc[r];
      } }
    { const float inv = __builtin_amdgcn_rcpf(o[8]);
      const u32x4 of = {cvtpk(o[0] * inv, o[1] * inv), cvtpk(o[2] * inv, o[3] * inv), cvtpk(o[4] * inv, o[5] * inv), cvtpk(o[6] * inv, o[7] * inv)};
      __builtin_amdgcn_raw_buffer_store_b128((u32x4){min(of[0], SENT - 1), min(of[1], SENT - 1), min(of[2], SENT - 1), min(of[3], SENT - 1)}, rs_os, lane * 16, 0, 16); }

    const __amdgpu_buffer_rsrc_t rs_o = __builtin_amdgcn_make_buffer_rsrc(ws + WS_O + (size_t)(4 * tb3 + kq) * 32768, 0, 32768, 0x00020000);
    const int oo = (g4 >> 1) * 2048 + ((g4 & 1) * 32 + t16) * 16;
    const LAS char* wa = smem + L_WO + ((g4 >> 1) * 32 + (g4 & 1) * 4 + ((lane & 15) >> 2)) * 64 + (lane & 3) * 8;
    const int h0 = (g4 & 1) << 5, h1 = h0 ^ 32;
    await(W_SEL, 1u);
    const unsigned skey = __builtin_amdgcn_readfirstlane(lw[W_SEL]);
    const int sel = 2047 - (int)(skey & 2047u);
    const bool own = (sel >> 7) == tb3;
    auto ysum = [&]() -> bool {
        const __amdgpu_buffer_rsrc_t rs_y = __builtin_amdgcn_make_buffer_rsrc(ws + WS_YP + (size_t)kq * 56 * 4096, 0, 56 * 4096, 0x00020000);
        float s0 = 0.f, s1 = 0.f; unsigned bad = 0;
        asm volatile("" ::: "memory");
#pragma unroll
        for (int i = 0; i < 14; ++i) {
            const u32x4 v = __builtin_amdgcn_raw_buffer_load_b128(rs_y, g4 * 4096 + cg3 * 256 + t16 * 16, i * 16384, 16);
            s0 += __uint_as_float(v[0]); s1 += __uint_as_float(v[2]); bad |= (v[1] ^ (unsigned)tag) | (v[3] ^ (unsigned)tag); }
        if (!__all(bad == 0) && --spin > 0) return false;
        auto a = __builtin_amdgcn_permlane16_swap(__float_as_uint(s0), __float_as_uint(s1), false, false);
        const float v = __uint_as_float(a[0]) + __uint_as_float(a[1]);
        auto b = __builtin_amdgcn_permlane32_swap(__float_as_uint(v), __float_as_uint(v), false, false);
        if (lane < 32) *(LAS float*)(smem + L_YS + kq * 128 + t16 * 8 + g4 * 4) = __uint_as_float(b[0]) + __uint_as_float(b[1]);
        arrive(W_Y);
        return true; };
    bool ydone = true;
    u32x4 eb[8];
    auto load_even = [&]() {
#pragma unroll
        for (int j = 0; j < 8; ++j) eb[j] = __builtin_amdgcn_raw_buffer_load_b128(rs_o, oo + hh * 256, j * 4096, OAUXL);
        __builtin_amdgcn_sched_barrier(0); };
    if (hh == 0) {
        asm volatile("s_waitcnt vmcnt(0)" ::: "memory");
        if (arrive(W_O) == 3) {
            await(W_PF, 4);
            if (lane == 0) __hip_atomic_store((GAS u64*)(ws + WS_OF + (tb * 16 + hp) * 128), tag, RLX_AGENT); }
        if (PRIO2 == 0) __builtin_amdgcn_s_setprio(0);
        if (ffn) {
            const __amdgpu_buffer_rsrc_t rs_p = __builtin_amdgcn_make_buffer_rsrc(ws + WS_P1 + (size_t)(fi >> 3) * 8192, 0, 8192, 0x00020000);
            const int po = (lane >> 3) * 1024 + ((lane >> 2) & 1) * 512 + (fi & 7) * 64 + (lane & 3) * 16;
            u32x4 pv = __builtin_amdgcn_raw_buffer_load_b128(rs_p, po, 0, 16);
            while (!__all(pv[1] == (unsigned)tag && pv[3] == (unsigned)tag) && spin > 0) {
                --spin; __builtin_amdgcn_s_sleep(8); asm volatile("" ::: "memory"); pv = __builtin_amdgcn_raw_buffer_load_b128(rs_p, po, 0, 16); }
            float hv[2];
#pragma unroll
            for (int j = 0; j < 2; ++j) {
                float v = __uint_as_float(pv[2 * j]); v += dppf<0x128>(v);
                auto a = __builtin_amdgcn_permlane16_swap(__float_as_uint(v), __float_as_uint(v), false, false); v = __uint_as_float(a[0]) + __uint_as_float(a[1]);
                auto b = __builtin_amdgcn_permlane32_swap(__float_as_uint(v), __float_as_uint(v), false, false);
                const float gs = bfr(__uint_as_float(b[0]) + __uint_as_float(b[1])), us = dppf<0x124>(gs);
                hv[j] = bfr(bfr(gs * sigmoidf_(gs)) * us); }
            float y0 = 0.f, y1 = 0.f;
#pragma unroll
            for (int f = 0; f < 8; ++f) {
                const float h = __int_as_float(__builtin_amdgcn_readlane(__float_as_int(hv[f & 1]), f >> 1));
                y0 = fmaf(h, bflo(wd2[f]), y0); y1 = fmaf(h, bfhi(wd2[f]), y1); }
            const __amdgpu_buffer_rsrc_t rs_w = __builtin_amdgcn_make_buffer_rsrc(ws + WS_YP + (size_t)fi * 4096, 0, 4096, 0x00020000);
            __builtin_amdgcn_raw_buffer_store_b128((u32x4){__float_as_uint(y0), (unsigned)tag, __float_as_uint(y1), (unsigned)tag}, rs_w, lane * 16, kq * 1024, 16);
        }
        __builtin_amdgcn_s_setprio(2);
        for (;;) {
            unsigned m;
            if (kq == 0) { m = (unsigned)__ballot(__hip_atomic_load((GAS u64*)(ws + WS_OF + (tb3 * 16 + t16) * 128), RLX_AGENT) == tag); lw[W_EM] = m; }
            else m = __builtin_amdgcn_readfirstlane(lw[W_EM]);
            if (m == ~0u || spin <= 0) break;
            --spin; __builtin_amdgcn_s_sleep(8); }
        asm volatile("" ::: "memory");
        load_even();
        while ((unsigned)__builtin_amdgcn_readfirstlane(lw[W_NEAR + kq]) == 0 && spin > 0) { --spin; __builtin_amdgcn_s_sleep(6); }
        asm volatile("" ::: "memory");
        if (own) ydone = ysum();
        __builtin_amdgcn_s_waitcnt(0x0f70);
    } else {
        await(W_EM, ~0u);
        asm volatile("" ::: "memory");
        load_even();
    }
    u32x4 wf[16];
#pragma unroll
    for (int j = 0; j < 8; ++j) { wf[2 * j] = trd2(wa + 1024 + j * 4096 + h0, 512); wf[2 * j + 1] = trd2(wa + 1024 + j * 4096 + h1, 512); }
    const float gt = bfhi((unsigned)(gg >> 32));
    float bb[8] = {bflo(xa[0]), bfhi(xa[0]), bflo(xa[1]), bfhi(xa[1]), bflo(xc[0]), bfhi(xc[0]), bflo(xc[1]), bfhi(xc[1])};
    const float gm = bfr(1.0f - gt);
#pragma unroll
    for (int r = 0; r < 8; ++r) { bb[r] = bfr(gm * bb[r]); asm volatile("" : "+v"(bb[r])); }
#pragma unroll
    for (int j = 0; j < 16; ++j) asm volatile("" : "+v"(wf[j]));
    if (hh == 1) { asm volatile("s_waitcnt vmcnt(8)" ::: "memory"); lw[W_GO + kq] = 1u; } else await(W_GO + kq, 1u);
    STAMP(19);
    u32x4 ob[8];
    f32x4 cg[4][2] = {};
    unsigned todo = 15;
    auto loadg = [&](int g) {
#pragma unroll
        for (int j = 2 * g; j < 2 * g + 2; ++j) { ob[j] = __builtin_amdgcn_raw_buffer_load_b128(rs_o, oo + 1024 + hh * 256, j * 4096, OAUXL); __builtin_amdgcn_sched_barrier(0); } };
    auto mulg = [&](int g) {
        const u32x4 a = ob[2 * g], b = ob[2 * g + 1];
        if (!__any(max(max(max(a[0], a[1]), max(a[2], a[3])), max(max(b[0], b[1]), max(b[2], b[3]))) == SENT) || spin <= 0) {
            todo &= ~(1u << g);
            cg[g][0] = mfma16(wf[4 * g], a, cg[g][0]); cg[g][1] = mfma16(wf[4 * g + 1], a, cg[g][1]);
            cg[g][0] = mfma16(wf[4 * g + 2], b, cg[g][0]); cg[g][1] = mfma16(wf[4 * g + 3], b, cg[g][1]); } };
    loadg(0); loadg(1); loadg(2); loadg(3);
    f32x4 e0 = {}, e1 = {};
#pragma unroll
    for (int j = 0; j < 8; ++j) { e0 = mfma16(trd2(wa + j * 4096 + h0, 512), eb[j], e0); e1 = mfma16(trd2(wa + j * 4096 + h1, 512), eb[j], e1); }
    asm volatile("" : "+v"(e0), "+v"(e1));
    __builtin_amdgcn_sched_barrier(0);
    mulg(0); mulg(1); mulg(2); mulg(3);
    while (todo) {
        --spin; asm volatile("" ::: "memory");
#pragma unroll
        for (int g = 0; g < 4; ++g) if (todo >> g & 1) loadg(g);
#pragma unroll
        for (int g = 0; g < 4; ++g) if (todo >> g & 1) mulg(g);
    }
    asm volatile("" : "+v"(cg[0][0]), "+v"(cg[0][1]), "+v"(cg[1][0]), "+v"(cg[1][1]), "+v"(cg[2][0]), "+v"(cg[2][1]), "+v"(cg[3][0]), "+v"(cg[3][1]));
    STAMP(20);
    if (own) {
        while (!ydone) ydone = ysum();
        if (((sel >> 4) & 7) == 2 * kq + hh) await(W_Y, 4); }
    if (own && tok == sel) {
        const float xo[8] = {bflo(xa[0]), bfhi(xa[0]), bflo(xa[1]), bfhi(xa[1]), bflo(xc[0]), bfhi(xc[0]), bflo(xc[1]), bfhi(xc[1])};
        const unsigned mono = (skey >> 11) & 0xffffu, rb = (mono & 0x8000u) ? (mono & 0x7fffu) : (~mono & 0xffffu);
        const float rsel = bfr(sigmoidf_(bflo(rb)));
        f32x4 y0 = {}, y1 = {};
#pragma unroll
        for (int i = 0; i < 4; ++i) { y0 += *(const LAS f32x4*)(smem + L_YS + i * 128 + g4 * 16); y1 += *(const LAS f32x4*)(smem + L_YS + i * 128 + 64 + g4 * 16); }
#pragma unroll
        for (int r = 0; r < 4; ++r) { bb[r] = bfr(gm * bfr(xo[r] + bfr(bfr(y0[r]) * rsel))); bb[4 + r] = bfr(gm * bfr(xo[4 + r] + bfr(bfr(y1[r]) * rsel))); }
    }
    const f32x4 c0 = e0 + ((cg[0][0] + cg[1][0]) + (cg[2][0] + cg[3][0])), c1 = e1 + ((cg[0][1] + cg[1][1]) + (cg[2][1] + cg[3][1]));
    f32x4 r0, r1;
#pragma unroll
    for (int r = 0; r < 4; ++r) { r0[r] = bfr(gt * bfr(c0[r])) + bb[r]; r1[r] = bfr(gt * bfr(c1[r])) + bb[4 + r]; }
    { const __amdgpu_buffer_rsrc_t rs_out = __builtin_amdgcn_make_buffer_rsrc(p.out, 0, 2u << 20, 0x00020000);
      __builtin_amdgcn_raw_buffer_store_b128(pack_pair(r0, r1), rs_out, tok * 1024 + 64 * cg3 + 32 * (g4 & 1) + 16 * (g4 >> 1), 0, 0); }
}

extern "C" void kernel_launch(void* const* d_in, const int* in_sizes, int n_in,
                              void* d_out, int out_size, void* d_ws, size_t ws_size,
                              hipStream_t stream) {
    Params p;
    p.x = (const uint16_t*)d_in[0]; p.wqkv = (const uint16_t*)d_in[1]; p.wo = (const uint16_t*)d_in[2];
    p.wr = (const uint16_t*)d_in[4]; p.wg = (const uint16_t*)d_in[5]; p.wu = (const uint16_t*)d_in[6];
    p.wd = (const uint16_t*)d_in[7]; p.gw = (const uint16_t*)d_in[8];
    p.out = (uint16_t*)d_out; p.ws = (char*)d_ws;
    mix_kernel<<<256, 512, 0, stream>>>(p);
}
```

```cpp
#include <hip/hip_runtime.h>
#include <hip/hip_bf16.h>
#include <stdint.h>

typedef __attribute__((ext_vector_type(8))) __bf16 bf16x8;
typedef __attribute__((ext_vector_type(16))) __bf16 bf16x16;
typedef __attribute__((ext_vector_type(4))) float f32x4;
typedef __attribute__((ext_vector_type(8))) float f32x8;
typedef __attribute__((ext_vector_type(16))) float f32x16;
typedef __attribute__((ext_vector_type(4))) unsigned u32x4;
typedef __attribute__((ext_vector_type(2))) unsigned u32x2;
typedef __attribute__((ext_vector_type(4))) short s16x4;
typedef unsigned long long u64;
#define LAS __attribute__((address_space(3)))
#define GAS __attribute__((address_space(1)))
#define RLX_AGENT __ATOMIC_RELAXED, __HIP_MEMORY_SCOPE_AGENT

constexpr size_t WS_KV = 0;
constexpr size_t WS_O = 4u << 20;
constexpr size_t WS_YP = 6u << 20;
constexpr size_t WS_G = 7u << 20;
constexpr size_t WS_RG = WS_G + 16384;
constexpr size_t WS_KVF = WS_RG + 2048;
constexpr size_t WS_OF = WS_KVF + 65536;
constexpr size_t WS_P1 = WS_OF + 65536;
constexpr size_t WS_EP = WS_P1 + 229376;

constexpr int SLOT = 24576, NSLOT = 5;
constexpr int L_WQ = SLOT * NSLOT;
constexpr int L_MISC = L_WQ + 32768;
constexpr int L_RK = L_MISC;
constexpr int L_W = L_MISC + 64;
constexpr int L_YS = L_MISC + 256;
constexpr int L_RW = L_MISC + 2048;
constexpr int LDS_BYTES = L_RW + 2048;
constexpr int L_CB = 0;
constexpr int L_Q = 57344;
constexpr int L_FS = 65536;
constexpr int L_EP = 69632;
constexpr int L_WO = 81920;
enum { W_Q = 0, W_KM = 2, W_C = 4, W_O = 6, W_F = 8, W_Y = 9, W_EM = 10, W_LM = 11, W_SEL = 12, W_PF = 13, W_NEAR = 16, W_GO = 20 };
constexpr unsigned SENT = 0xffffffffu;
constexpr int SPIN_CAP = 200000;
#define POLL 0
#define KVAUX 16
#define OAUXL 16
#define PRIO2 2
#define PFQ 0
#define ROT 4

#define STAMP(i)
#define CSTAMP(i)
#define STAMP_DECL
#define STAMP_OUT

struct Params {
    const uint16_t* x; const uint16_t* wqkv; const uint16_t* wo; const uint16_t* wr;
    const uint16_t* wg; const uint16_t* wu; const uint16_t* wd; const uint16_t* gw;
    uint16_t* out; char* ws;
};

__device__ __forceinline__ float bflo(unsigned u) { return __uint_as_float(u << 16); }
__device__ __forceinline__ float bfhi(unsigned u) { return __uint_as_float(u & 0xffff0000u); }
__device__ __forceinline__ unsigned cvtpk(float lo, float hi) {
    typedef __attribute__((ext_vector_type(2))) float f2; typedef __attribute__((ext_vector_type(2))) __bf16 b2;
    return __builtin_bit_cast(unsigned, __builtin_convertvector((f2){lo, hi}, b2));
}
__device__ __forceinline__ float bfr(float v) { return bflo(cvtpk(v, 0.f) & 0xffffu); }
__device__ __forceinline__ float sigmoidf_(float v) { return __builtin_amdgcn_rcpf(1.0f + __builtin_amdgcn_exp2f(v * -1.4426950408889634f)); }
__device__ __forceinline__ float dot2(unsigned a, unsigned b, float c) {
    typedef __attribute__((ext_vector_type(2))) __bf16 b2;
    return __builtin_amdgcn_fdot2_f32_bf16(__builtin_bit_cast(b2, a), __builtin_bit_cast(b2, b), c, false);
}
__device__ __forceinline__ unsigned lo2(unsigned a, unsigned b) { return __builtin_amdgcn_perm(b, a, 0x05040100u); }
__device__ __forceinline__ unsigned hi2(unsigned a, unsigned b) { return __builtin_amdgcn_perm(b, a, 0x07060302u); }
__device__ __forceinline__ f32x4 mfma16(u32x4 a, u32x4 b, f32x4 c) {
    return __builtin_amdgcn_mfma_f32_16x16x32_bf16(__builtin_bit_cast(bf16x8, a), __builtin_bit_cast(bf16x8, b), c, 0, 0, 0);
}
__device__ __forceinline__ u32x2 trd(const LAS char* p) {
    return __builtin_bit_cast(u32x2, __builtin_amdgcn_ds_read_tr16_b64_v4i16((LAS s16x4*)p));
}
__device__ __forceinline__ u32x4 trd2(const LAS char* p, int d) { u32x2 a = trd(p), b = trd(p + d); return (u32x4){a[0], a[1], b[0], b[1]}; }
__device__ __forceinline__ void dma16(const void* sbase, unsigned voff, unsigned lds) {
    unsigned keep;
    asm volatile("s_mov_b32 %0, m0\n\ts_mov_b32 m0, %3\n\ts_nop 0\n\tglobal_load_lds_dwordx4 %1, %2\n\ts_mov_b32 m0, %0"
                 : "=&s"(keep) : "v"(voff), "s"(sbase), "s"(lds) : "memory");
}
__device__ __forceinline__ void dma16_half(const void* sbase, unsigned voff, unsigned lds) {
    unsigned keep;
    asm volatile("s_mov_b32 %0, m0\n\ts_mov_b32 m0, %3\n\ts_mov_b32 exec_hi, 0\n\tglobal_load_lds_dwordx4 %1, %2\n\ts_mov_b32 exec_hi, -1\n\ts_mov_b32 m0, %0"
                 : "=&s"(keep) : "v"(voff), "s"(sbase), "s"(lds) : "memory");
}
template <int N> __device__ __forceinline__ void wait_bar() {
    asm volatile("" ::: "memory"); __builtin_amdgcn_s_waitcnt(0xc07f);
    asm volatile("s_waitcnt vmcnt(%0) lgkmcnt(0)\n\ts_barrier" :: "n"(N) : "memory"); }
template <int N> __device__ __forceinline__ void vm_bar() { asm volatile("s_waitcnt vmcnt(%0)\n\ts_barrier" :: "n"(N) : "memory"); }
__device__ __forceinline__ void lds_bar() { asm volatile("s_waitcnt lgkmcnt(0)\n\ts_barrier" ::: "memory"); }
__device__ __forceinline__ void bare_bar() { asm volatile("s_barrier" ::: "memory"); }
template <int CTRL> __device__ __forceinline__ float dppf(float v) {
    return __int_as_float(__builtin_amdgcn_update_dpp(0, __float_as_int(v), CTRL, 0xf, 0xf, true));
}
template <int CTRL> __device__ __forceinline__ unsigned dppu(unsigned v) {
    return (unsigned)__builtin_amdgcn_update_dpp(0, (int)v, CTRL, 0xf, 0xf, true);
}
__device__ __forceinline__ float row_sum16(float v) {
    v += dppf<0x128>(v); v += dppf<0x124>(v); v += dppf<0x122>(v); v += dppf<0x121>(v); return v;
}
__device__ __forceinline__ float wave_sum(float v) {
    v = row_sum16(v);
    auto a = __builtin_amdgcn_permlane16_swap(__float_as_uint(v), __float_as_uint(v), false, false);
    v = __uint_as_float(a[0]) + __uint_as_float(a[1]);
    auto b = __builtin_amdgcn_permlane32_swap(__float_as_uint(v), __float_as_uint(v), false, false);
    return __uint_as_float(b[0]) + __uint_as_float(b[1]);
}
__device__ __forceinline__ unsigned wave_maxu(unsigned v) {
    v = max(v, dppu<0x128>(v)); v = max(v, dppu<0x124>(v)); v = max(v, dppu<0x122>(v)); v = max(v, dppu<0x121>(v));
    auto a = __builtin_amdgcn_permlane16_swap(v, v, false, false); v = max(a[0], a[1]);
    auto b = __builtin_amdgcn_permlane32_swap(v, v, false, false); return max(b[0], b[1]);
}
__device__ __forceinline__ u32x4 pack_pair(f32x4 a, f32x4 b) {
    auto x = __builtin_amdgcn_permlane16_swap(cvtpk(a[0], a[1]), cvtpk(b[0], b[1]), false, false);
    auto y = __builtin_amdgcn_permlane16_swap(cvtpk(a[2], a[3]), cvtpk(b[2], b[3]), false, false);
    return (u32x4){x[0], y[0], x[1], y[1]};
}
#define STR2(x) #x
#define STR(x) STR2(x)
#define EX(r) "v_exp_f32 v" STR(r) ", v" STR(r) "\n\t"
#define QKM(S, K, Q) "v_mfma_f32_32x32x16_bf16 " S ", " K ", " Q ", 0\n\t"
#define PVM(O, V, P) "v_smfmac_f32_32x32x32_bf16 " O ", " V ", " P ", %[idx]\n\t"
#define UNIT_A(O, V0, V1, MID) EX(194) EX(195) EX(196) MID EX(197) EX(198) EX(199) EX(200) EX(201) PVM(O, V0, "v[192:199]") \
    EX(202) EX(203) EX(204) EX(205) EX(206) EX(207) EX(208) EX(209) PVM(O, V1, "v[200:207]")
#define UNIT_B(O, V0, V1, MID) EX(210) EX(211) EX(212) MID EX(213) EX(214) EX(215) EX(216) EX(217) PVM(O, V0, "v[208:215]") \
    EX(218) EX(219) EX(220) EX(221) EX(222) EX(223) EX(192) EX(193) PVM(O, V1, "v[216:223]")
#define UNIT_B_LAST(O, V0, V1) EX(210) EX(211) EX(212) EX(213) EX(214) EX(215) EX(216) EX(217) PVM(O, V0, "v[208:215]") \
    EX(218) EX(219) EX(220) EX(221) EX(222) EX(223) "s_nop 1\n\t" PVM(O, V1, "v[216:223]")
#define SA "v[192:207]"
#define SB "v[208:223]"
#define S_CLOB "v192", "v193", "v194", "v195", "v196", "v197", "v198", "v199", "v200", "v201", "v202", "v203", "v204", "v205", "v206", "v207", \
    "v208", "v209", "v210", "v211", "v212", "v213", "v214", "v215", "v216", "v217", "v218", "v219", "v220", "v221", "v222", "v223"
#define LOADT(J, K, V0, V1) "s_xor_b32 %[t], %[sb], " J "\n\t" \
    "buffer_load_dwordx4 " K ", %[ko], %[rs], %[t] offen sc1\n\t" \
    "s_mov_b64 exec, %[vm]\n\t" \
    "buffer_load_dwordx4 " V0 ", %[ko], %[rs], %[t] offen offset:1024 sc1\n\t" \
    "buffer_load_dwordx4 " V1 ", %[ko], %[rs], %[t] offen offset:1280 sc1\n\t" \
    "s_mov_b64 exec, -1\n\t"
#define GROUP "s_add_u32 %[gi], %[gi], 0x8000\n\ts_and_b32 %[gi], %[gi], 0x18000\n\ts_xor_b32 %[sb], %[so], %[gi]\n\t" \
    "s_lshr_b32 %[t], %[sb], 13\n\ts_and_b32 %[t], %[t], 12\n\ts_lshl_b32 %[t], 15, %[t]\n\t" \
    "s_andn2_b32 %[u], %[t], %[rdy]\n\ts_cbranch_scc0 3f\n\t" \
    "2:\n\ts_waitcnt vmcnt(0)\n\t" \
    "v_cmp_eq_u64 vcc, %[tag], %[fl]\n\ts_nop 4\n\ts_and_b32 %[rdy], vcc_lo, 0xffff\n\t" \
    "s_andn2_b32 %[u], %[t], %[rdy]\n\ts_cbranch_scc0 3f\n\t" \
    "global_load_dwordx2 %[fl], %[fo], %[fb] sc1\n\t" \
    "s_sub_i32 %[spin], %[spin], 1\n\ts_cmp_gt_i32 %[spin], 0\n\ts_cbranch_scc1 2b\n\t" \
    "3:\n\t"
#define TILE(B, BN, LD) \
    UNIT_A("%[o0]", "%[" B "v0]", "%[" B "v1]", QKM(SB, "%[" B "k]", "%[q1]") LD) \
    UNIT_B("%[o1]", "%[" B "v0]", "%[" B "v1]", QKM(SA, "%[" B "k]", "%[q2]")) \
    UNIT_A("%[o2]", "%[" B "v0]", "%[" B "v1]", QKM(SB, "%[" B "k]", "%[q3]")) \
    UNIT_B("%[o3]", "%[" B "v0]", "%[" B "v1]", "s_waitcnt vmcnt(6)\n\t" QKM(SA, "%[" BN "k]", "%[q0]"))
#define WODMA "s_cmp_lg_u32 %[hh], 0\n\ts_cbranch_scc1 5f\n\ts_mov_b32 m0, %[wl]\n\ts_add_u32 %[wl], %[wl], 0x400\n\t" \
    "global_load_lds_dwordx4 %[wv], %[wp]\n\tv_add_u32 %[wv], 0x4000, %[wv]\n\t5:\n\t"
#define FPRE "s_cmp_lg_u32 %[hh], 0\n\ts_cbranch_scc1 5f\n\tglobal_load_dword %[fd], %[fa], off\n\t5:\n\t"
#define LOADB(J, B) LOADT(J, "%[" B "k]", "%[" B "v0]", "%[" B "v1]")

__global__ __launch_bounds__(512, 2) void mix_kernel(Params p) {
    __shared__ __attribute__((aligned(16))) char smem_[LDS_BYTES];
    LAS char* smem = (LAS char*)smem_;
    const unsigned lds0 = (unsigned)(uintptr_t)smem;
    const int tid = threadIdx.x, lane = tid & 63;
    const int w = __builtin_amdgcn_readfirstlane(tid >> 6);
    const int xcd = blockIdx.x & 7, jj = blockIdx.x >> 3;
    const int tb = 4 * (xcd >> 1) + (jj & 3), hp = 8 * (xcd & 1) + (jj >> 2);
    const int kq = w & 3, hh = w >> 2, head = 2 * hp + hh;
    const int g4 = lane >> 4, t16 = lane & 15;
    char* const ws = p.ws;
    const __amdgpu_buffer_rsrc_t rs_kv = __builtin_amdgcn_make_buffer_rsrc(ws + WS_KV + (size_t)head * 131072 + kq * 2048, 0, 131072 - kq * 2048, 0x00020000);
    int spin = SPIN_CAP;
    STAMP_DECL; STAMP(0);
    asm volatile("" :: "s"(p.x), "s"(p.wqkv), "s"(p.wo), "s"(p.wr), "s"(p.wg), "s"(p.wu), "s"(p.wd), "s"(p.gw), "s"(p.out), "s"(p.ws));
    STAMP(24);

    const int tokr = 128 * tb + 8 * hp + w;
    u64 ep;
    asm volatile("s_load_dwordx2 %0, %1, 0x0" : "=s"(ep) : "s"(ws + WS_EP) : "memory");
    if (w < 2) dma16(w ? p.gw : p.wr, lane * 16, lds0 + L_RW + w * 1024);
    unsigned pq = 0;
    if (PFQ && w == 7) asm volatile("global_load_dword %0, %1, %2" : "=v"(pq) : "v"((jj * 16 + (lane >> 2)) * 3072 + (xcd & 1) * 512 + (lane & 3) * 128), "s"(p.wqkv) : "memory");
    u32x4 rx;
    asm volatile("global_load_dwordx4 %0, %1, %2" : "=v"(rx) : "v"(lane * 16), "s"(p.x + (size_t)tokr * 512) : "memory");
    unsigned xo0, xo1, wo0;
    { const int t0 = 16 * w + (lane >> 3), t1 = t0 + 8;
      xo0 = t0 * 1024 + (((lane & 7) ^ ((t0 >> 1) & 7)) << 4);
      xo1 = t1 * 1024 + (((lane & 7) ^ ((t1 >> 1) & 7)) << 4);
      const int k0 = 16 * (w & 3) + (lane >> 2), pc = ((lane & 3) ^ (((k0 >> 3) & 1) << 1)) << 4;
      wo0 = k0 * 3072 + 1024 + (w >> 2) * 1024 + pc; }
    const char* xb = (const char*)p.x + (size_t)tb * 131072;
    const char* wb = (const char*)p.wqkv + hp * 64;
    const int rot = ROT == 0 ? 0 : ROT == 1 ? (2 * (jj & 3) + (jj >> 2)) & 7 : ROT == 2 ? (((jj & 3) + (jj >> 2)) & 1) * 4 : ((jj & 3) + (jj >> 2) * 2 + (jj >> 3)) & 3;
    const int rx4 = ROT == 5 ? rot ^ (((xcd ^ (xcd >> 1)) & 1) << 2) : rot;
    auto chunk = [&](int c) { return ROT >= 4 ? c ^ rx4 : (c + rot) & 7; };
    auto issue = [&](int c, int i) {
        const unsigned s = lds0 + (c % NSLOT) * SLOT; const int cc = chunk(c);
        if (i == 0) dma16(xb + cc * 128, xo0, s + w * 2048);
        if (i == 1) dma16(wb + cc * 196608, wo0, s + 16384 + w * 1024);
        if (i == 2) dma16(xb + cc * 128, xo1, s + w * 2048 + 1024);
    };
    const __amdgpu_buffer_rsrc_t rs_os = __builtin_amdgcn_make_buffer_rsrc(ws + WS_O + (size_t)(4 * tb + kq) * 32768 + head * 1024, 0, 1024, 0x00020000);
#pragma unroll
    for (int i = 0; i < 3; ++i) issue(0, i);
    if (tid < 32) *(LAS unsigned*)(smem + L_W + 4 * tid) = 0u;
    asm volatile("s_waitcnt vmcnt(3) lgkmcnt(0)\n\ts_barrier" : "+v"(rx), "+s"(ep) : "v"(pq) : "memory");
    const u64 tag = ep + 1;
    unsigned gbits;
    {
            const u32x4 rw = *(const LAS u32x4*)(smem + L_RW + lane * 16), rgw = *(const LAS u32x4*)(smem + L_RW + 1024 + lane * 16);
            float ra = 0.f, ga = 0.f;
#pragma unroll
            for (int j = 0; j < 4; ++j) {
                const float a = bflo(rx[j]), b = bfhi(rx[j]);
                ra = fmaf(a, bflo(rw[j]), ra); ra = fmaf(b, bfhi(rw[j]), ra);
                const unsigned pr = cvtpk(a * bflo(rgw[j]), b * bfhi(rgw[j]));
                ga += bflo(pr); ga += bfhi(pr); }
            ra = wave_sum(ra); ga = wave_sum(ga);
            const unsigned rb = cvtpk(ra, 0.f) & 0xffffu;
            const unsigned key = (((rb & 0x8000u) ? (~rb & 0xffffu) : (rb | 0x8000u)) << 11) | (unsigned)(2047 - tokr);
            gbits = cvtpk(sigmoidf_(bfr(ga)), 0.f) & 0xffffu;
            if (lane == 0) *(LAS unsigned*)(smem + L_RK + 4 * w) = key; }
#pragma unroll
    for (int c = 1; c < 3; ++c) {
#pragma unroll
        for (int i = 0; i < 3; ++i) issue(c, i);
        if (c == 1) { lds_bar();
            if (w == 0) {
                const unsigned kk = wave_maxu(*(const LAS unsigned*)(smem + L_RK + 4 * (lane & 7)));
                if (lane == 0) __hip_atomic_store((GAS u64*)(ws + WS_RG) + blockIdx.x, ((u64)kk << 37) | (tag & ((1ull << 37) - 1)), RLX_AGENT);
            }
        }
    }
    const int fi = min((int)blockIdx.x, 223); const bool ffn = blockIdx.x < 224;
    const __amdgpu_buffer_rsrc_t rs_rg = __builtin_amdgcn_make_buffer_rsrc(ws + WS_RG, 0, 2048, 0x00020000);
    STAMP(1);
    u32x4 kf, vc, qf;
    const int frow = 64 * (fi & 7) + 16 * kq + (lane >> 3);
    {   f32x4 aq[2] = {}, ak[2] = {}, av[2] = {};
        const int fsw = (t16 >> 1) & 7;
        const int xa = (32 * kq + t16) * 128 + ((g4 ^ fsw) << 4);
        const int wa = g4 * 512 + ((lane & 15) >> 2) * 64 + ((hh ^ (g4 & 1)) << 5) + (lane & 3) * 8;
        u32x4 fx[8][4], fw[2][4];
        auto frag = [&](int c, int i) {
            const LAS char* s = smem + (c % NSLOT) * SLOT; const int kk = i >> 2, j = i & 3;
            if (j == 1 || j == 2) fx[c][2 * kk + j - 1] = *(const LAS u32x4*)(s + (xa ^ (kk * 64)) + (j - 1) * 2048);
            else fw[c & 1][2 * kk + (j ? 1 : 0)] = trd2(s + 16384 + wa + (j ? 4096 : 0) + kk * 2048, 256); };
        wait_bar<6>();
        STAMP(3); CSTAMP(24);
#pragma unroll
        for (int i = 0; i < 8; ++i) { frag(0, i); if (i < 3) issue(3, i); __builtin_amdgcn_sched_barrier(0); }
#pragma unroll
        for (int c = 0; c < 8; ++c) {
            if (c < 5) vm_bar<6>(); else if (c == 5) vm_bar<5>(); else if (c == 6) vm_bar<4>();
            if (c == 1) { STAMP(6); CSTAMP(25); }
            if (c == 2) { STAMP(11); CSTAMP(26); }
            if (c == 3) { STAMP(7); CSTAMP(27); }
            if (c == 5) { STAMP(9); CSTAMP(28); }
            if (c == 6) { STAMP(10); }
            __builtin_amdgcn_sched_barrier(0);
#pragma unroll
            for (int i = 0; i < 8; ++i) {
                const int kk = i >> 2, j = i & 3; const u32x4 xx = fx[c][2 * kk + (j & 1)], ww = fw[c & 1][2 * kk + (j >> 1)];
                if (j < 2) ak[j] = mfma16(ww, xx, ak[j]); else av[j - 2] = mfma16(xx, ww, av[j - 2]);
                if (c < 7 && i < 4) { frag(c + 1, 2 * i); frag(c + 1, 2 * i + 1); }
                if (c < 4 && (i & 1) == 0 && i < 6) issue(c + 4, i >> 1);
                if ((c == 4 || c == 5) && (i & 3) == 1) { const int n = (2 * (c - 4) + (i >> 2)) ^ (PFQ ? 0 : rot & 3);
                    dma16(wb + (64 * w + 16 * n) * 3072, (lane >> 2) * 3072 + (((lane & 3) ^ ((lane >> 4) & 2)) << 4), lds0 + L_WQ + (4 * w + n) * 1024); }
                __builtin_amdgcn_sched_barrier(0);
            }
        }
        kf = pack_pair(ak[0], ak[1]); vc = pack_pair(av[0], av[1]);
        asm volatile("s_waitcnt vmcnt(0)\n\ts_barrier" : "+v"(kf), "+v"(vc) :: "memory");
        __builtin_amdgcn_raw_buffer_store_b128(kf, rs_kv, lane * 16, tb * 8192, 16);
        __builtin_amdgcn_raw_buffer_store_b128(vc, rs_kv, lane * 16 + 1024, tb * 8192, 16);
        STAMP(12); CSTAMP(29);
        u32x4 fq[8][2];
        auto fragq = [&](int c) { const LAS char* s = smem + L_WQ + wa + chunk(c) * 4096; fq[c][0] = trd2(s, 256); fq[c][1] = trd2(s + 2048, 256); };
        fragq(0); fragq(1); fragq(2);
        __builtin_amdgcn_sched_barrier(0);
#pragma unroll
        for (int c = 0; c < 8; ++c) {
            aq[0] = mfma16(fq[c][0], fx[c][0], aq[0]); aq[1] = mfma16(fq[c][0], fx[c][1], aq[1]);
            if (c + 3 < 8) fragq(c + 3);
            __builtin_amdgcn_sched_barrier(0);
            aq[0] = mfma16(fq[c][1], fx[c][2], aq[0]); aq[1] = mfma16(fq[c][1], fx[c][3], aq[1]);
            __builtin_amdgcn_sched_barrier(0);
        }
        constexpr float QS = 0.25f * 1.4426950408889634f;
        qf = pack_pair(aq[0] * QS, aq[1] * QS);
        *(LAS u32x4*)(smem + L_Q + w * 1024 + lane * 16) = qf;
        __builtin_amdgcn_sched_barrier(0);
    }
    STAMP(4);

    volatile LAS unsigned* lw = (volatile LAS unsigned*)(smem + L_W);
    auto arrive = [&](int word) -> unsigned {
        unsigned t = 0; asm volatile("s_waitcnt lgkmcnt(0)" ::: "memory");
        if (lane == 0) t = __hip_atomic_fetch_add((LAS unsigned*)(smem + L_W) + word, 1u, __ATOMIC_RELAXED, __HIP_MEMORY_SCOPE_WORKGROUP);
        return __builtin_amdgcn_readfirstlane(t); };
    auto await = [&](int word, unsigned n) {
        while ((unsigned)__builtin_amdgcn_readfirstlane(lw[word]) < n && spin > 0) { --spin; __builtin_amdgcn_s_sleep(2); }
        asm volatile("" ::: "memory"); };
    const unsigned pat = ((lane & 11) == 0) ? 0x3f803f80u : 0u;
    u32x4 xv0, xv1;
#pragma unroll
    for (int j = 0; j < 4; ++j) { auto r = __builtin_amdgcn_permlane16_swap(vc[j], pat, false, false); xv0[j] = r[0]; xv1[j] = r[1]; }
    f32x16 o0 = {}, o1 = {}, o2 = {}, o3 = {};
    const int idx = 0xdddd;
    const char* kvf = ws + WS_KVF + head * 2048;
    asm volatile("" : "+v"(o0), "+v"(o1), "+v"(o2), "+v"(o3), "+v"(xv0), "+v"(xv1));
    u32x4 rg0, rg1, wv4[4];
    if (hh == 1) {
        if (kq == 0) { rg0 = __builtin_amdgcn_raw_buffer_load_b128(rs_rg, lane * 32, 0, 16); rg1 = __builtin_amdgcn_raw_buffer_load_b128(rs_rg, lane * 32 + 16, 0, 16); }
        asm volatile("" ::: "memory");
#pragma unroll
        for (int j = 0; j < 4; ++j) wv4[j] = *(const u32x4*)(((j & 1) ? p.wu : p.wg) + (size_t)(frow + 8 * (j >> 1)) * 1792 + 64 * (fi >> 3) + 8 * (lane & 7));
        asm volatile("s_waitcnt vmcnt(4)" ::: "memory");
    } else asm volatile("s_waitcnt vmcnt(0)" ::: "memory");
    if (arrive(W_Q + hh) == 3 && lane == 0) asm volatile("global_store_dwordx2 %0, %1, %2 sc1" :: "v"(tb * 128), "v"(tag), "s"(kvf) : "memory");
    if (hh == 1) __builtin_amdgcn_raw_buffer_store_b128((u32x4){SENT, SENT, SENT, SENT}, rs_os, lane * 16, 0, 16);
    await(W_Q + hh, 4);
    STAMP(13);
    const u32x4 q0 = qf;
    const u32x4 q1 = *(const LAS u32x4*)(smem + L_Q + (hh * 4 + ((kq + 1) & 3)) * 1024 + lane * 16);
    const u32x4 q2 = *(const LAS u32x4*)(smem + L_Q + (hh * 4 + ((kq + 2) & 3)) * 1024 + lane * 16);
    const u32x4 q3 = *(const LAS u32x4*)(smem + L_Q + (hh * 4 + ((kq + 3) & 3)) * 1024 + lane * 16);
    asm volatile(
        "s_nop 1\n\t" QKM(SA, "%[k]", "%[q0]") "s_nop 11\n\t" EX(192) EX(193)
        UNIT_A("%[o0]", "%[v0]", "%[v1]", QKM(SB, "%[k]", "%[q1]"))
        UNIT_B("%[o1]", "%[v0]", "%[v1]", QKM(SA, "%[k]", "%[q2]"))
        UNIT_A("%[o2]", "%[v0]", "%[v1]", QKM(SB, "%[k]", "%[q3]"))
        UNIT_B_LAST("%[o3]", "%[v0]", "%[v1]") "s_nop 11"
        : [o0] "+v"(o0), [o1] "+v"(o1), [o2] "+v"(o2), [o3] "+v"(o3)
        : [k] "v"(kf), [v0] "v"(xv0), [v1] "v"(xv1), [q0] "v"(q0), [q1] "v"(q1), [q2] "v"(q2), [q3] "v"(q3), [idx] "v"(idx)
        : S_CLOB);
    STAMP(15);
    __builtin_amdgcn_s_setprio(2);
    if (w == 4) {
        const unsigned tl = (unsigned)tag, th = (unsigned)(tag >> 32) & 31u;
        auto fresh = [&]() { return __all((((rg0[0] ^ tl) | (rg0[2] ^ tl) | (rg1[0] ^ tl) | (rg1[2] ^ tl)) | (((rg0[1] ^ th) | (rg0[3] ^ th) | (rg1[1] ^ th) | (rg1[3] ^ th)) & 31u)) == 0); };
        if (!fresh()) do {
            --spin; asm volatile("" ::: "memory");
            rg0 = __builtin_amdgcn_raw_buffer_load_b128(rs_rg, lane * 32, 0, 16); rg1 = __builtin_amdgcn_raw_buffer_load_b128(rs_rg, lane * 32 + 16, 0, 16);
        } while (!fresh() && spin > 0);
        const unsigned kk = __builtin_amdgcn_readfirstlane(wave_maxu(max(max(rg0[1], rg0[3]), max(rg1[1], rg1[3])) >> 5));
        lw[W_SEL] = kk | 0x80000000u;
        if (blockIdx.x == 0 && lane == 0) __hip_atomic_store((GAS u64*)(ws + WS_EP), tag, RLX_AGENT);
    }
    unsigned xs0, xs1;
    if (hh == 1) {
        await(W_SEL, 1u);
        const uint16_t* xr = p.x + (size_t)(2047 - (int)(__builtin_amdgcn_readfirstlane(lw[W_SEL]) & 2047u)) * 512 + frow;
        xs0 = xr[0]; xs1 = xr[8]; }
    { const unsigned need = (15u << (tb & 12)) & ~(1u << tb);
      for (;;) {
          unsigned m;
          if (kq == 0) { m = (unsigned)__ballot(__hip_atomic_load((GAS u64*)(kvf + t16 * 128), RLX_AGENT) == tag) & 0xffffu; lw[W_KM + hh] = m; }
          else { m = __builtin_amdgcn_readfirstlane(lw[W_KM + hh]); __builtin_amdgcn_s_sleep(1); }
          if ((m & need) == need || spin <= 0) break;
          --spin; }
      __builtin_amdgcn_s_waitcnt(0x0f70);
      asm volatile("" : "+v"(xs0), "+v"(xs1) :: "memory"); }
    STAMP(2);
    u32x4 yk, yv0, yv1, zk, zv0, zv1, uk, uv0, uv1;
    yv0 = yv1 = zv0 = zv1 = uv0 = uv1 = (u32x4){pat, pat, pat, pat};
    { const int s1 = (tb ^ 1) * 8192, s2 = (tb ^ 2) * 8192, s3 = (tb ^ 3) * 8192;
      yk = __builtin_amdgcn_raw_buffer_load_b128(rs_kv, lane * 16, s1, KVAUX);
      if ((lane & 16) == 0) {
          yv0 = __builtin_amdgcn_raw_buffer_load_b128(rs_kv, lane * 16 + 1024, s1, KVAUX); yv1 = __builtin_amdgcn_raw_buffer_load_b128(rs_kv, lane * 16 + 1280, s1, KVAUX);
          zv0 = __builtin_amdgcn_raw_buffer_load_b128(rs_kv, lane * 16 + 1024, s2, KVAUX); zv1 = __builtin_amdgcn_raw_buffer_load_b128(rs_kv, lane * 16 + 1280, s2, KVAUX);
          uv0 = __builtin_amdgcn_raw_buffer_load_b128(rs_kv, lane * 16 + 1024, s3, KVAUX); uv1 = __builtin_amdgcn_raw_buffer_load_b128(rs_kv, lane * 16 + 1280, s3, KVAUX); }
      zk = __builtin_amdgcn_raw_buffer_load_b128(rs_kv, lane * 16, s2, KVAUX);
      uk = __builtin_amdgcn_raw_buffer_load_b128(rs_kv, lane * 16, s3, KVAUX); }
    u64 fl = __hip_atomic_load((GAS u64*)(kvf + t16 * 128), RLX_AGENT);
    if (hh == 1) {
        const unsigned xp = xs0 | (xs1 << 16);
        float a[16];
#pragma unroll
        for (int j = 0; j < 4; ++j) {
            a[2 * j] = dot2(lo2(wv4[0][j], wv4[2][j]), xp, 0.f); a[2 * j + 1] = dot2(hi2(wv4[0][j], wv4[2][j]), xp, 0.f);
            a[8 + 2 * j] = dot2(lo2(wv4[1][j], wv4[3][j]), xp, 0.f); a[9 + 2 * j] = dot2(hi2(wv4[1][j], wv4[3][j]), xp, 0.f); }
#pragma unroll
        for (int j = 0; j < 8; ++j) { auto r = __builtin_amdgcn_permlane32_swap(__float_as_uint(a[j]), __float_as_uint(a[j + 8]), false, false); a[j] = __uint_as_float(r[0]) + __uint_as_float(r[1]); }
#pragma unroll
        for (int j = 0; j < 4; ++j) { auto r = __builtin_amdgcn_permlane16_swap(__float_as_uint(a[j]), __float_as_uint(a[j + 4]), false, false); a[j] = __uint_as_float(r[0]) + __uint_as_float(r[1]); }
        const f32x4 sm = {a[0] + dppf<0x128>(a[0]), a[1] + dppf<0x128>(a[1]), a[2] + dppf<0x128>(a[2]), a[3] + dppf<0x128>(a[3])};
        if ((lane & 8) == 0) *(LAS f32x4*)(smem + L_FS + kq * 512 + (lane >> 5) * 256 + (lane & 7) * 32 + (g4 & 1) * 16) = sm;
        arrive(W_F);
        if (ffn && kq < 2) {
            await(W_F, 4);
            float ps = 0.f;
#pragma unroll
            for (int i = 0; i < 4; ++i) ps += *(const LAS float*)(smem + L_FS + i * 512 + (kq * 64 + lane) * 4);
            __hip_atomic_store((GAS u64*)(ws + WS_P1) + fi * 128 + kq * 64 + lane, ((u64)(unsigned)tag << 32) | __float_as_uint(ps), RLX_AGENT); }
    }
    STAMP(8);
    asm volatile("" :: "v"(yk), "v"(yv0), "v"(yv1), "v"(zk), "v"(zv0), "v"(zv1), "v"(uk), "v"(uv0), "v"(uv1), "v"(fl) : "memory");
    if (hh == 1) { asm volatile("s_waitcnt vmcnt(0)" ::: "memory"); arrive(W_PF); }
    if (lane == 0) __hip_atomic_store((GAS u64*)(ws + WS_G) + tokr, ((u64)gbits << 48) | (tag & ((1ull << 48) - 1)), RLX_AGENT);
    const int tb3 = 2 * xcd + (jj & 1), cg3 = jj >> 1;
    STAMP(16);
    CSTAMP(30);
    { int gi = 0, cnt = 4, sb, t, u;
      const u64 vm = 0x0000ffff0000ffffull;
      int rdy = (int)(unsigned)__ballot(fl == tag) & 0xffff, m0s;
      unsigned wl = lds0 + L_WO + kq * 8192, wv = (128 * kq + (lane >> 2)) * 1024 + (((lane & 3) ^ ((lane >> 3) & 2)) << 4);
      asm volatile(
          "s_mov_b32 %[m0s], m0\n\ts_setprio 1\n\t" QKM(SA, "%[yk]", "%[q0]") "s_nop 11\n\t" EX(192) EX(193)
          "1:\n\t"
          TILE("y", "z", GROUP LOADB("0", "x"))
          TILE("z", "u", WODMA LOADB("0x2000", "y"))
          TILE("u", "x", WODMA LOADB("0x4000", "z"))
          "s_sub_u32 %[cnt], %[cnt], 1\n\ts_cmp_eq_u32 %[cnt], 0\n\ts_cbranch_scc1 4f\n\t"
          TILE("x", "y", LOADB("0x6000", "u"))
          "s_branch 1b\n\t"
          "4:\n\ts_mov_b32 m0, %[m0s]\n\ts_setprio 2\n\ts_waitcnt vmcnt(0)\n\ts_nop 11"
          : [o0] "+v"(o0), [o1] "+v"(o1), [o2] "+v"(o2), [o3] "+v"(o3),
            [xk] "+v"(kf), [xv0] "+v"(xv0), [xv1] "+v"(xv1), [yk] "+v"(yk), [yv0] "+v"(yv0), [yv1] "+v"(yv1),
            [zk] "+v"(zk), [zv0] "+v"(zv0), [zv1] "+v"(zv1), [uk] "+v"(uk), [uv0] "+v"(uv0), [uv1] "+v"(uv1),
            [gi] "+s"(gi), [cnt] "+s"(cnt), [rdy] "+s"(rdy), [spin] "+s"(spin),
            [sb] "=&s"(sb), [t] "=&s"(t), [u] "=&s"(u), [fl] "+v"(fl), [m0s] "=&s"(m0s), [wl] "+s"(wl), [wv] "+v"(wv)
          : [q0] "v"(q0), [q1] "v"(q1), [q2] "v"(q2), [q3] "v"(q3), [idx] "v"(idx), [ko] "v"(lane * 16),
            [rs] "s"(rs_kv), [vm] "s"(vm), [fo] "v"(t16 * 128), [fb] "s"(kvf), [tag] "s"(tag), [so] "s"(tb * 8192),
            [wp] "s"((const char*)p.wo + cg3 * 64), [hh] "s"(hh)
          : S_CLOB, "vcc", "scc", "memory"); }
    __builtin_amdgcn_s_waitcnt(0x0f70);
    STAMP(17);
    unsigned wd2[8];
    if (hh == 1) { __builtin_amdgcn_s_setprio(3); lw[W_NEAR + kq] = 1u; }
    else {
#pragma unroll
        for (int f = 0; f < 8; ++f) wd2[f] = *(const unsigned*)(p.wd + (size_t)(8 * fi + f) * 512 + 128 * kq + 2 * lane);
    }
    CSTAMP(31);
    const int tok = 128 * tb3 + 32 * kq + 16 * hh + t16;
    const GAS u64* gp = (const GAS u64*)(ws + WS_G) + tok;
    u64 gg = __hip_atomic_load(gp, RLX_AGENT);
    const u32x2 xa = *(const u32x2*)(p.x + (size_t)tok * 512 + 32 * cg3 + 4 * g4), xc = *(const u32x2*)(p.x + (size_t)tok * 512 + 32 * cg3 + 16 + 4 * g4);
    { auto put = [&](const f32x16& o, int j) {
          LAS char* d = smem + L_CB + ((hh * 4 + ((kq + j) & 3)) * 3 + (j - 1)) * 2304;
          *(LAS f32x4*)(d + lane * 16) = (f32x4){o[0], o[1], o[2], o[3]};
          *(LAS f32x4*)(d + 1024 + lane * 16) = (f32x4){o[4], o[5], o[6], o[7]};
          *(LAS float*)(d + 2048 + lane * 4) = o[8]; };
      put(o1, 1); put(o2, 2); put(o3, 3); }
    arrive(W_C + hh); await(W_C + hh, 4);
    STAMP(18);
    while (__any((gg ^ tag) << 16 != 0) && spin > 0) { --spin; gg = __hip_atomic_load(gp, RLX_AGENT); }
    asm volatile("" :: "v"(xa), "v"(xc));
    float o[9];
#pragma unroll
    for (int r = 0; r < 9; ++r) o[r] = o0[r];
#pragma unroll
    for (int j = 0; j < 3; ++j) {
        const LAS char* d = smem + L_CB + (w * 3 + j) * 2304;
        const f32x4 a = *(const LAS f32x4*)(d + lane * 16), b = *(const LAS f32x4*)(d + 1024 + lane * 16);
#pragma unroll
        for (int r = 0; r < 4; ++r) { o[r] += a[r]; o[4 + r] += b[r]; }
        o[8] += *(const LAS float*)(d + 2048 + lane * 4);
    }
    { float mag = 0.f;
#pragma unroll
      for (int r = 0; r < 8; ++r) mag += fabsf(o[r]);
      const bool safe = o[8] >= 9.313225746154785e-10f && o[8] <= 1.8446744073709552e19f && mag <= 3.0e38f;
      if (!__all(safe)) {
          f32x16 acc = {}; float m = -INFINITY;
          u32x4 v0 = {pat, pat, pat, pat}, v1 = v0;
          const __amdgpu_buffer_rsrc_t rs_all = __builtin_amdgcn_make_buffer_rsrc(ws + WS_KV + (size_t)head * 131072, 0, 131072, 0x00020000);
          for (int t = 0; t < 64; ++t) {
              const u32x4 kk = __builtin_amdgcn_raw_buffer_load_b128(rs_all, lane * 16, t * 2048, 16);
              if ((lane & 16) == 0) { v0 = __builtin_amdgcn_raw_buffer_load_b128(rs_all, lane * 16 + 1024, t * 2048, 16); v1 = __builtin_amdgcn_raw_buffer_load_b128(rs_all, lane * 16 + 1280, t * 2048, 16); }
              f32x16 s = __builtin_amdgcn_mfma_f32_32x32x16_bf16(__builtin_bit_cast(bf16x8, kk), __builtin_bit_cast(bf16x8, q0), (f32x16){}, 0, 0, 0);
              float tm = s[0];
#pragma unroll
              for (int r = 1; r < 16; ++r) tm = fmaxf(tm, s[r]);
              auto sw = __builtin_amdgcn_permlane32_swap(__float_as_uint(tm), __float_as_uint(tm), false, false);
              const float mn = fmaxf(m, fmaxf(__uint_as_float(sw[0]), __uint_as_float(sw[1])));
              const float al = __builtin_amdgcn_exp2f(m - mn); m = mn;
#pragma unroll
              for (int r = 0; r < 16; ++r) { s[r] = __builtin_amdgcn_exp2f(s[r] - mn); acc[r] *= al; }
              acc = __builtin_amdgcn_smfmac_f32_32x32x32_bf16(__builtin_bit_cast(bf16x8, v0), __builtin_bit_cast(bf16x16, __builtin_shufflevector(s, s, 0, 1, 2, 3, 4, 5, 6, 7)), acc, idx, 0, 0);
              acc = __builtin_amdgcn_smfmac_f32_32x32x32_bf16(__builtin_bit_cast(bf16x8, v1), __builtin_bit_cast(bf16x16, __builtin_shufflevector(s, s, 8, 9, 10, 11, 12, 13, 14, 15)), acc, idx, 0, 0);
          }
#pragma unroll
          for (int r = 0; r < 9; ++r) o[r] = acc[r];
      } }
    { const float inv = __builtin_amdgcn_rcpf(o[8]);
      const u32x4 of = {cvtpk(o[0] * inv, o[1] * inv), cvtpk(o[2] * inv, o[3] * inv), cvtpk(o[4] * inv, o[5] * inv), cvtpk(o[6] * inv, o[7] * inv)};
      __builtin_amdgcn_raw_buffer_store_b128((u32x4){min(of[0], SENT - 1), min(of[1], SENT - 1), min(of[2], SENT - 1), min(of[3], SENT - 1)}, rs_os, lane * 16, 0, 16); }

    const __amdgpu_buffer_rsrc_t rs_o = __builtin_amdgcn_make_buffer_rsrc(ws + WS_O + (size_t)(4 * tb3 + kq) * 32768, 0, 32768, 0x00020000);
    const int oo = (g4 >> 1) * 2048 + ((g4 & 1) * 32 + t16) * 16;
    const LAS char* wa = smem + L_WO + ((g4 >> 1) * 32 + (g4 & 1) * 4 + ((lane & 15) >> 2)) * 64 + (lane & 3) * 8;
    const int h0 = (g4 & 1) << 5, h1 = h0 ^ 32;
    await(W_SEL, 1u);
    const unsigned skey = __builtin_amdgcn_readfirstlane(lw[W_SEL]);
    const int sel = 2047 - (int)(skey & 2047u);
    const bool own = (sel >> 7) == tb3;
    auto ysum = [&]() -> bool {
        const __amdgpu_buffer_rsrc_t rs_y = __builtin_amdgcn_make_buffer_rsrc(ws + WS_YP + (size_t)kq * 56 * 4096, 0, 56 * 4096, 0x00020000);
        float s0 = 0.f, s1 = 0.f; unsigned bad = 0;
        asm volatile("" ::: "memory");
#pragma unroll
        for (int i = 0; i < 14; ++i) {
            const u32x4 v = __builtin_amdgcn_raw_buffer_load_b128(rs_y, g4 * 4096 + cg3 * 256 + t16 * 16, i * 16384, 16);
            s0 += __uint_as_float(v[0]); s1 += __uint_as_float(v[2]); bad |= (v[1] ^ (unsigned)tag) | (v[3] ^ (unsigned)tag); }
        if (!__all(bad == 0) && --spin > 0) return false;
        auto a = __builtin_amdgcn_permlane16_swap(__float_as_uint(s0), __float_as_uint(s1), false, false);
        const float v = __uint_as_float(a[0]) + __uint_as_float(a[1]);
        auto b = __builtin_amdgcn_permlane32_swap(__float_as_uint(v), __float_as_uint(v), false, false);
        if (lane < 32) *(LAS float*)(smem + L_YS + kq * 128 + t16 * 8 + g4 * 4) = __uint_as_float(b[0]) + __uint_as_float(b[1]);
        arrive(W_Y);
        return true; };
    bool ydone = true;
    u32x4 eb[8];
    auto load_even = [&]() {
#pragma unroll
        for (int j = 0; j < 8; ++j) eb[j] = __builtin_amdgcn_raw_buffer_load_b128(rs_o, oo + hh * 256, j * 4096, OAUXL);
        __builtin_amdgcn_sched_barrier(0); };
    if (hh == 0) {
        asm volatile("s_waitcnt vmcnt(0)" ::: "memory");
        if (arrive(W_O) == 3) {
            await(W_PF, 4);
            if (lane == 0) __hip_atomic_store((GAS u64*)(ws + WS_OF + (tb * 16 + hp) * 128), tag, RLX_AGENT); }
        if (PRIO2 == 0) __builtin_amdgcn_s_setprio(0);
        if (ffn) {
            const __amdgpu_buffer_rsrc_t rs_p = __builtin_amdgcn_make_buffer_rsrc(ws + WS_P1 + (size_t)(fi >> 3) * 8192, 0, 8192, 0x00020000);
            const int po = (lane >> 3) * 1024 + ((lane >> 2) & 1) * 512 + (fi & 7) * 64 + (lane & 3) * 16;
            u32x4 pv = __builtin_amdgcn_raw_buffer_load_b128(rs_p, po, 0, 16);
            while (!__all(pv[1] == (unsigned)tag && pv[3] == (unsigned)tag) && spin > 0) {
                --spin; __builtin_amdgcn_s_sleep(8); asm volatile("" ::: "memory"); pv = __builtin_amdgcn_raw_buffer_load_b128(rs_p, po, 0, 16); }
            float hv[2];
#pragma unroll
            for (int j = 0; j < 2; ++j) {
                float v = __uint_as_float(pv[2 * j]); v += dppf<0x128>(v);
                auto a = __builtin_amdgcn_permlane16_swap(__float_as_uint(v), __float_as_uint(v), false, false); v = __uint_as_float(a[0]) + __uint_as_float(a[1]);
                auto b = __builtin_amdgcn_permlane32_swap(__float_as_uint(v), __float_as_uint(v), false, false);
                const float gs = bfr(__uint_as_float(b[0]) + __uint_as_float(b[1])), us = dppf<0x124>(gs);
                hv[j] = bfr(gs * sigmoidf_(gs)) * us; }
            const unsigned hp2 = cvtpk(hv[0], hv[1]);
            float y0 = 0.f, y1 = 0.f;
#pragma unroll
            for (int f = 0; f < 4; ++f) {
                const unsigned h = __builtin_amdgcn_readlane(hp2, f);
                y0 = dot2(lo2(wd2[2 * f], wd2[2 * f + 1]), h, y0); y1 = dot2(hi2(wd2[2 * f], wd2[2 * f + 1]), h, y1); }
            const __amdgpu_buffer_rsrc_t rs_w = __builtin_amdgcn_make_buffer_rsrc(ws + WS_YP + (size_t)fi * 4096, 0, 4096, 0x00020000);
            __builtin_amdgcn_raw_buffer_store_b128((u32x4){__float_as_uint(y0), (unsigned)tag, __float_as_uint(y1), (unsigned)tag}, rs_w, lane * 16, kq * 1024, 16);
        }
        __builtin_amdgcn_s_setprio(2);
        for (;;) {
            unsigned m;
            if (kq == 0) { m = (unsigned)__ballot(__hip_atomic_load((GAS u64*)(ws + WS_OF + (tb3 * 16 + t16) * 128), RLX_AGENT) == tag); lw[W_EM] = m; }
            else m = __builtin_amdgcn_readfirstlane(lw[W_EM]);
            if (m == ~0u || spin <= 0) break;
            --spin; __builtin_amdgcn_s_sleep(8); }
        asm volatile("" ::: "memory");
        load_even();
        while ((unsigned)__builtin_amdgcn_readfirstlane(lw[W_NEAR + kq]) == 0 && spin > 0) { --spin; __builtin_amdgcn_s_sleep(6); }
        asm volatile("" ::: "memory");
        if (own) ydone = ysum();
        __builtin_amdgcn_s_waitcnt(0x0f70);
    } else {
        await(W_EM, ~0u);
        asm volatile("" ::: "memory");
        load_even();
    }
    u32x4 wf[16];
#pragma unroll
    for (int j = 0; j < 8; ++j) { wf[2 * j] = trd2(wa + 1024 + j * 4096 + h0, 512); wf[2 * j + 1] = trd2(wa + 1024 + j * 4096 + h1, 512); }
    const float gt = bfhi((unsigned)(gg >> 32));
    float bb[8] = {bflo(xa[0]), bfhi(xa[0]), bflo(xa[1]), bfhi(xa[1]), bflo(xc[0]), bfhi(xc[0]), bflo(xc[1]), bfhi(xc[1])};
    const float gm = bfr(1.0f - gt);
#pragma unroll
    for (int r = 0; r < 8; ++r) { bb[r] = bfr(gm * bb[r]); asm volatile("" : "+v"(bb[r])); }
#pragma unroll
    for (int j = 0; j < 16; ++j) asm volatile("" : "+v"(wf[j]));
    if (hh == 1) { asm volatile("s_waitcnt vmcnt(8)" ::: "memory"); lw[W_GO + kq] = 1u; } else await(W_GO + kq, 1u);
    STAMP(19);
    u32x4 ob[8];
    auto load_odd = [&](int from) {
#pragma unroll
        for (int j = 0; j < 8; ++j) if (j >= from) { ob[j] = __builtin_amdgcn_raw_buffer_load_b128(rs_o, oo + 1024 + hh * 256, j * 4096, OAUXL); __builtin_amdgcn_sched_barrier(0); } };
    load_odd(0);
    f32x4 c0 = {}, c1 = {};
#pragma unroll
    for (int j = 0; j < 8; ++j) { c0 = mfma16(trd2(wa + j * 4096 + h0, 512), eb[j], c0); c1 = mfma16(trd2(wa + j * 4096 + h1, 512), eb[j], c1); }
    asm volatile("" : "+v"(c0), "+v"(c1));
    __builtin_amdgcn_sched_barrier(0);
#pragma unroll
    for (int g = 0; g < 4; ++g) {
        auto missing = [&]() { const u32x4 a = ob[2 * g], b = ob[2 * g + 1];
            return __any(max(max(max(a[0], a[1]), max(a[2], a[3])), max(max(b[0], b[1]), max(b[2], b[3]))) == SENT); };
        if (missing()) do { --spin; asm volatile("" ::: "memory"); load_odd(2 * g); } while (missing() && spin > 0);
        c0 = mfma16(wf[4 * g], ob[2 * g], c0); c1 = mfma16(wf[4 * g + 1], ob[2 * g], c1);
        c0 = mfma16(wf[4 * g + 2], ob[2 * g + 1], c0); c1 = mfma16(wf[4 * g + 3], ob[2 * g + 1], c1);
    }
    STAMP(20);
    if (own) {
        while (!ydone) ydone = ysum();
        if (((sel >> 4) & 7) == 2 * kq + hh) await(W_Y, 4); }
    if (own && tok == sel) {
        const float xo[8] = {bflo(xa[0]), bfhi(xa[0]), bflo(xa[1]), bfhi(xa[1]), bflo(xc[0]), bfhi(xc[0]), bflo(xc[1]), bfhi(xc[1])};
        const unsigned mono = (skey >> 11) & 0xffffu, rb = (mono & 0x8000u) ? (mono & 0x7fffu) : (~mono & 0xffffu);
        const float rsel = bfr(sigmoidf_(bflo(rb)));
        f32x4 y0 = {}, y1 = {};
#pragma unroll
        for (int i = 0; i < 4; ++i) { y0 += *(const LAS f32x4*)(smem + L_YS + i * 128 + g4 * 16); y1 += *(const LAS f32x4*)(smem + L_YS + i * 128 + 64 + g4 * 16); }
#pragma unroll
        for (int r = 0; r < 4; ++r) { bb[r] = bfr(gm * bfr(xo[r] + bfr(bfr(y0[r]) * rsel))); bb[4 + r] = bfr(gm * bfr(xo[4 + r] + bfr(bfr(y1[r]) * rsel))); }
    }
    f32x4 r0, r1;
#pragma unroll
    for (int r = 0; r < 4; r += 2) {
        const unsigned a0 = cvtpk(c0[r], c0[r + 1]), a1 = cvtpk(c1[r], c1[r + 1]);
        const unsigned t0 = cvtpk(gt * bflo(a0), gt * bfhi(a0)), t1 = cvtpk(gt * bflo(a1), gt * bfhi(a1));
        r0[r] = bflo(t0) + bb[r]; r0[r + 1] = bfhi(t0) + bb[r + 1]; r1[r] = bflo(t1) + bb[4 + r]; r1[r + 1] = bfhi(t1) + bb[5 + r]; }
    { const __amdgpu_buffer_rsrc_t rs_out = __builtin_amdgcn_make_buffer_rsrc(p.out, 0, 2u << 20, 0x00020000);
      __builtin_amdgcn_raw_buffer_store_b128(pack_pair(r0, r1), rs_out, tok * 1024 + 64 * cg3 + 32 * (g4 & 1) + 16 * (g4 >> 1), 0, 0); }
}

extern "C" void kernel_launch(void* const* d_in, const int* in_sizes, int n_in,
                              void* d_out, int out_size, void* d_ws, size_t ws_size,
                              hipStream_t stream) {
    Params p;
    p.x = (const uint16_t*)d_in[0]; p.wqkv = (const uint16_t*)d_in[1]; p.wo = (const uint16_t*)d_in[2];
    p.wr = (const uint16_t*)d_in[4]; p.wg = (const uint16_t*)d_in[5]; p.wu = (const uint16_t*)d_in[6];
    p.wd = (const uint16_t*)d_in[7]; p.gw = (const uint16_t*)d_in[8];
    p.out = (uint16_t*)d_out; p.ws = (char*)d_ws;
    mix_kernel<<<256, 512, 0, stream>>>(p);
}
```

```cpp
#include <hip/hip_runtime.h>
#include <hip/hip_bf16.h>
#include <stdint.h>

typedef __attribute__((ext_vector_type(8))) __bf16 bf16x8;
typedef __attribute__((ext_vector_type(16))) __bf16 bf16x16;
typedef __attribute__((ext_vector_type(4))) float f32x4;
typedef __attribute__((ext_vector_type(8))) float f32x8;
typedef __attribute__((ext_vector_type(16))) float f32x16;
typedef __attribute__((ext_vector_type(4))) unsigned u32x4;
typedef __attribute__((ext_vector_type(2))) unsigned u32x2;
typedef __attribute__((ext_vector_type(4))) short s16x4;
typedef unsigned long long u64;
#define LAS __attribute__((address_space(3)))
#define GAS __attribute__((address_space(1)))
#define RLX_AGENT __ATOMIC_RELAXED, __HIP_MEMORY_SCOPE_AGENT

constexpr size_t WS_KV = 0;
constexpr size_t WS_O = 4u << 20;
constexpr size_t WS_YP = 6u << 20;
constexpr size_t WS_G = 7u << 20;
constexpr size_t WS_RG = WS_G + 16384;
constexpr size_t WS_KVF = WS_RG + 2048;
constexpr size_t WS_OF = WS_KVF + 65536;
constexpr size_t WS_P1 = WS_OF + 65536;
constexpr size_t WS_EP = WS_P1 + 229376;

constexpr int SLOT = 24576, NSLOT = 5;
constexpr int L_WQ = SLOT * NSLOT;
constexpr int L_MISC = L_WQ + 32768;
constexpr int L_RK = L_MISC;
constexpr int L_W = L_MISC + 64;
constexpr int L_YS = L_MISC + 256;
constexpr int L_RW = L_MISC + 2048;
constexpr int LDS_BYTES = L_RW + 2048;
constexpr int L_CB = 0;
constexpr int L_Q = 57344;
constexpr int L_FW = 65536;
constexpr int L_WO = 81920;
constexpr int L_WD = 114688;
enum { W_Q = 0, W_KM = 2, W_C = 4, W_O = 6, W_F = 8, W_Y = 9, W_EM = 10, W_D = 24, W_SEL = 12, W_PF = 13, W_WQ = 14, W_NEAR = 16, W_GO = 20, W_PRE = 28 };
constexpr unsigned SENT = 0xffffffffu;
__device__ __forceinline__ u32x4 unsent(u32x4 v) { return (u32x4){min(v[0], SENT - 1), min(v[1], SENT - 1), min(v[2], SENT - 1), min(v[3], SENT - 1)}; }
__device__ __forceinline__ unsigned max4(u32x4 v) { return max(max(v[0], v[1]), max(v[2], v[3])); }
constexpr int SPIN_CAP = 200000;
#define POLL 0
#define KVAUX 16
#define OAUXL 16
#define NPOLL 1
#define GOPRIO 3
#define WROT 1
#define DPOS 3
#define YSL 32
#define WDR 0
#define FR 1
#define CPF 1
#define ROT 4

#define STAMP(i)
#define CSTAMP(i)
#define STAMP_DECL
#define STAMP_OUT

struct Params {
    const uint16_t* x; const uint16_t* wqkv; const uint16_t* wo; const uint16_t* wr;
    const uint16_t* wg; const uint16_t* wu; const uint16_t* wd; const uint16_t* gw;
    uint16_t* out; char* ws;
};

__device__ __forceinline__ float bflo(unsigned u) { return __uint_as_float(u << 16); }
__device__ __forceinline__ float bfhi(unsigned u) { return __uint_as_float(u & 0xffff0000u); }
__device__ __forceinline__ unsigned cvtpk(float lo, float hi) {
    typedef __attribute__((ext_vector_type(2))) float f2; typedef __attribute__((ext_vector_type(2))) __bf16 b2;
    return __builtin_bit_cast(unsigned, __builtin_convertvector((f2){lo, hi}, b2));
}
__device__ __forceinline__ float bfr(float v) { return bflo(cvtpk(v, 0.f) & 0xffffu); }
__device__ __forceinline__ float sigmoidf_(float v) { return __builtin_amdgcn_rcpf(1.0f + __builtin_amdgcn_exp2f(v * -1.4426950408889634f)); }
__device__ __forceinline__ float dot2(unsigned a, unsigned b, float c) {
    typedef __attribute__((ext_vector_type(2))) __bf16 b2;
    return __builtin_amdgcn_fdot2_f32_bf16(__builtin_bit_cast(b2, a), __builtin_bit_cast(b2, b), c, false);
}
__device__ __forceinline__ unsigned lo2(unsigned a, unsigned b) { return __builtin_amdgcn_perm(b, a, 0x05040100u); }
__device__ __forceinline__ unsigned hi2(unsigned a, unsigned b) { return __builtin_amdgcn_perm(b, a, 0x07060302u); }
__device__ __forceinline__ f32x4 mfma16(u32x4 a, u32x4 b, f32x4 c) {
    return __builtin_amdgcn_mfma_f32_16x16x32_bf16(__builtin_bit_cast(bf16x8, a), __builtin_bit_cast(bf16x8, b), c, 0, 0, 0);
}
__device__ __forceinline__ u32x2 trd(const LAS char* p) {
    return __builtin_bit_cast(u32x2, __builtin_amdgcn_ds_read_tr16_b64_v4i16((LAS s16x4*)p));
}
__device__ __forceinline__ u32x4 trd2(const LAS char* p, int d) { u32x2 a = trd(p), b = trd(p + d); return (u32x4){a[0], a[1], b[0], b[1]}; }
__device__ __forceinline__ void dma16(const void* sbase, unsigned voff, unsigned lds) {
    unsigned keep;
    asm volatile("s_mov_b32 %0, m0\n\ts_mov_b32 m0, %3\n\ts_nop 0\n\tglobal_load_lds_dwordx4 %1, %2\n\ts_mov_b32 m0, %0"
                 : "=&s"(keep) : "v"(voff), "s"(sbase), "s"(lds) : "memory");
}
__device__ __forceinline__ void dma16_half(const void* sbase, unsigned voff, unsigned lds) {
    unsigned keep;
    asm volatile("s_mov_b32 %0, m0\n\ts_mov_b32 m0, %3\n\ts_mov_b32 exec_hi, 0\n\tglobal_load_lds_dwordx4 %1, %2\n\ts_mov_b32 exec_hi, -1\n\ts_mov_b32 m0, %0"
                 : "=&s"(keep) : "v"(voff), "s"(sbase), "s"(lds) : "memory");
}
template <int N> __device__ __forceinline__ void wait_bar() {
    asm volatile("" ::: "memory"); __builtin_amdgcn_s_waitcnt(0xc07f);
    asm volatile("s_waitcnt vmcnt(%0) lgkmcnt(0)\n\ts_barrier" :: "n"(N) : "memory"); }
template <int N> __device__ __forceinline__ void vm_bar() { asm volatile("s_waitcnt vmcnt(%0)\n\ts_barrier" :: "n"(N) : "memory"); }
__device__ __forceinline__ void lds_bar() { asm volatile("s_waitcnt lgkmcnt(0)\n\ts_barrier" ::: "memory"); }
__device__ __forceinline__ void bare_bar() { asm volatile("s_barrier" ::: "memory"); }
template <int CTRL> __device__ __forceinline__ float dppf(float v) {
    return __int_as_float(__builtin_amdgcn_update_dpp(0, __float_as_int(v), CTRL, 0xf, 0xf, true));
}
template <int CTRL> __device__ __forceinline__ unsigned dppu(unsigned v) {
    return (unsigned)__builtin_amdgcn_update_dpp(0, (int)v, CTRL, 0xf, 0xf, true);
}
__device__ __forceinline__ float row_sum16(float v) {
    v += dppf<0x128>(v); v += dppf<0x124>(v); v += dppf<0x122>(v); v += dppf<0x121>(v); return v;
}
__device__ __forceinline__ float wave_sum(float v) {
    v = row_sum16(v);
    auto a = __builtin_amdgcn_permlane16_swap(__float_as_uint(v), __float_as_uint(v), false, false);
    v = __uint_as_float(a[0]) + __uint_as_float(a[1]);
    auto b = __builtin_amdgcn_permlane32_swap(__float_as_uint(v), __float_as_uint(v), false, false);
    return __uint_as_float(b[0]) + __uint_as_float(b[1]);
}
__device__ __forceinline__ unsigned wave_maxu(unsigned v) {
    v = max(v, dppu<0x128>(v)); v = max(v, dppu<0x124>(v)); v = max(v, dppu<0x122>(v)); v = max(v, dppu<0x121>(v));
    auto a = __builtin_amdgcn_permlane16_swap(v, v, false, false); v = max(a[0], a[1]);
    auto b = __builtin_amdgcn_permlane32_swap(v, v, false, false); return max(b[0], b[1]);
}
__device__ __forceinline__ u32x4 pack_pair(f32x4 a, f32x4 b) {
    auto x = __builtin_amdgcn_permlane16_swap(cvtpk(a[0], a[1]), cvtpk(b[0], b[1]), false, false);
    auto y = __builtin_amdgcn_permlane16_swap(cvtpk(a[2], a[3]), cvtpk(b[2], b[3]), false, false);
    return (u32x4){x[0], y[0], x[1], y[1]};
}
#define STR2(x) #x
#define STR(x) STR2(x)
#define EX(r) "v_exp_f32 v" STR(r) ", v" STR(r) "\n\t"
#define QKM(S, K, Q) "v_mfma_f32_32x32x16_bf16 " S ", " K ", " Q ", 0\n\t"
#define PVM(O, V, P) "v_smfmac_f32_32x32x32_bf16 " O ", " V ", " P ", %[idx]\n\t"
#define UNIT_AX(O, V0, V1, MID, X) EX(194) EX(195) EX(196) MID EX(197) EX(198) EX(199) EX(200) EX(201) PVM(O, V0, "v[192:199]") \
    EX(202) EX(203) EX(204) EX(205) X EX(206) EX(207) EX(208) EX(209) PVM(O, V1, "v[200:207]")
#define UNIT_BX(O, V0, V1, MID, X) EX(210) EX(211) EX(212) MID EX(213) EX(214) EX(215) EX(216) EX(217) PVM(O, V0, "v[208:215]") \
    EX(218) EX(219) EX(220) EX(221) X EX(222) EX(223) EX(192) EX(193) PVM(O, V1, "v[216:223]")
#define UNIT_B_LAST(O, V0, V1, X) EX(210) EX(211) EX(212) EX(213) EX(214) EX(215) EX(216) EX(217) PVM(O, V0, "v[208:215]") \
    EX(218) EX(219) EX(220) EX(221) X EX(222) EX(223) "s_nop 1\n\t" PVM(O, V1, "v[216:223]")
#define UNIT_A(O, V0, V1, MID) UNIT_AX(O, V0, V1, MID, "")
#define UNIT_B(O, V0, V1, MID) UNIT_BX(O, V0, V1, MID, "")
#define O0 "v[128:143]"
#define O1 "v[144:159]"
#define O2 "v[160:175]"
#define O3 "v[176:191]"
#define O_OPS "+{v[128:143]}"(o0), "+{v[144:159]}"(o1), "+{v[160:175]}"(o2), "+{v[176:191]}"(o3)
#define PUT(J, LO, HI, L) "ds_write_b128 %[pa" #J "], " LO "\n\tds_write_b128 %[pa" #J "], " HI " offset:1024\n\tds_write_b32 %[pb" #J "], " L "\n\t"
#define SA "v[192:207]"
#define SB "v[208:223]"
#define S_CLOB "v192", "v193", "v194", "v195", "v196", "v197", "v198", "v199", "v200", "v201", "v202", "v203", "v204", "v205", "v206", "v207", \
    "v208", "v209", "v210", "v211", "v212", "v213", "v214", "v215", "v216", "v217", "v218", "v219", "v220", "v221", "v222", "v223"
#define LOADT(J, K, V0, V1) "s_xor_b32 %[t], %[sb], " J "\n\t" \
    "buffer_load_dwordx4 " K ", %[ko], %[rs], %[t] offen sc1\n\t" \
    "s_mov_b64 exec, %[vm]\n\t" \
    "buffer_load_dwordx4 " V0 ", %[ko], %[rs], %[t] offen offset:1024 sc1\n\t" \
    "buffer_load_dwordx4 " V1 ", %[ko], %[rs], %[t] offen offset:1280 sc1\n\t" \
    "s_mov_b64 exec, -1\n\t"
#define GROUP "s_xor_b32 %[sb], %[sb], %[gi]\n\ts_xor_b32 %[gi], %[gi], 0x10000\n\t" \
    "s_cmp_eq_u32 %[rdy], 0xffff\n\ts_cbranch_scc1 3f\n\t" \
    "s_lshr_b32 %[t], %[sb], 13\n\ts_and_b32 %[t], %[t], 12\n\ts_lshl_b32 %[t], 15, %[t]\n\t" \
    "s_andn2_b32 %[u], %[t], %[rdy]\n\ts_cbranch_scc0 3f\n\t" \
    "2:\n\ts_waitcnt vmcnt(0)\n\t" \
    "v_cmp_eq_u64 vcc, %[tag], %[fl]\n\ts_nop 4\n\ts_and_b32 %[rdy], vcc_lo, 0xffff\n\t" \
    "s_andn2_b32 %[u], %[t], %[rdy]\n\ts_cbranch_scc0 3f\n\t" \
    "global_load_dwordx2 %[fl], %[fo], %[fb] sc1\n\t" \
    "s_sub_i32 %[spin], %[spin], 1\n\ts_cmp_gt_i32 %[spin], 0\n\ts_cbranch_scc1 2b\n\t" \
    "3:\n\t"
#define TILE3(B, BN, LD, W, LD3) \
    UNIT_A(O1, "%[" B "v0]", "%[" B "v1]", QKM(SB, "%[" B "k]", "%[q2]") LD) \
    UNIT_B(O2, "%[" B "v0]", "%[" B "v1]", QKM(SA, "%[" B "k]", "%[q3]")) \
    UNIT_A(O3, "%[" B "v0]", "%[" B "v1]", QKM(SB, "%[" B "k]", "%[q0]")) \
    UNIT_B(O0, "%[" B "v0]", "%[" B "v1]", W QKM(SA, "%[" BN "k]", "%[q1]") LD3)
#define TILE2(B, BN, LD, LD3) TILE3(B, BN, LD, "s_waitcnt vmcnt(6)\n\t", LD3)
#define TILE(B, BN, LD) TILE2(B, BN, LD, "")
#define WO1 "s_lshr_b32 %[t], %[wi], 4\n\ts_add_u32 m0, %[wl], %[t]\n\ts_add_u32 %[u], %[wi], 0x4000\n\t" \
    "buffer_load_dwordx4 %[wv], %[rw], %[wi] offen lds\n\ts_and_b32 %[wi], %[u], 0x1c000\n\t"
#define FW1(O, V, P) "s_add_u32 m0, %[fw], " O "\n\ts_nop 0\n\tglobal_load_lds_dwordx4 " V ", " P "\n\t"
#define WODMA "s_cmp_lg_u32 %[hh], 0\n\ts_cbranch_scc1 5f\n\t" WO1 WO1 "s_cmp_lg_u32 %[cnt], " STR(WDR) "\n\ts_cbranch_scc1 8f\n\t" \
    FW1("0xc000", "%[ko]", "%[wdp]") FW1("0xc400", "%[k1]", "%[wdp]") "8:\n\ts_cmp_lg_u32 %[cnt], " STR(FR) "\n\ts_cbranch_scc1 5f\n\t" \
    FW1("0", "%[f0]", "%[wgp]") FW1("0x2000", "%[f0]", "%[wup]") FW1("0x400", "%[f1]", "%[wgp]") FW1("0x2400", "%[f1]", "%[wup]") "5:\n\t"
#define WAITX(C) "s_cmp_eq_u32 %[cnt], " C "\n\ts_cbranch_scc1 7f\n\ts_waitcnt vmcnt(6)\n\t7:\n\ts_waitcnt vmcnt(12)\n\t"
#define LOADB(J, B) LOADT(J, "%[" B "k]", "%[" B "v0]", "%[" B "v1]")

__global__ __launch_bounds__(512, 2) void mix_kernel(Params p) {
    __shared__ __attribute__((aligned(16))) char smem_[LDS_BYTES];
    LAS char* smem = (LAS char*)smem_;
    const unsigned lds0 = (unsigned)(uintptr_t)smem;
    const int tid = threadIdx.x, lane = tid & 63;
    const int w = __builtin_amdgcn_readfirstlane(tid >> 6);
    const int xcd = blockIdx.x & 7, jj = blockIdx.x >> 3;
    const int tb = 4 * (xcd >> 1) + (jj & 3), hp = 8 * (xcd & 1) + (jj >> 2);
    const int kq = w & 3, hh = w >> 2, head = 2 * hp + hh;
    const int g4 = lane >> 4, t16 = lane & 15;
    char* const ws = p.ws;
    const __amdgpu_buffer_rsrc_t rs_kv = __builtin_amdgcn_make_buffer_rsrc(ws + WS_KV + (size_t)head * 131072 + kq * 2048, 0, 131072 - kq * 2048, 0x00020000);
    int spin = SPIN_CAP;
    STAMP_DECL; STAMP(0); CSTAMP(14);
    unsigned xo0, xo1, wo0;
    { const int t0 = 16 * w + (lane >> 3), t1 = t0 + 8;
      xo0 = t0 * 1024 + (((lane & 7) ^ ((t0 >> 1) & 7)) << 4);
      xo1 = t1 * 1024 + (((lane & 7) ^ ((t1 >> 1) & 7)) << 4);
      const int k0 = 16 * (w & 3) + (lane >> 2), pc = ((lane & 3) ^ (((k0 >> 3) & 1) << 1)) << 4;
      wo0 = k0 * 3072 + 1024 + (w >> 2) * 1024 + pc; }
    asm volatile("" : "+v"(xo0), "+v"(xo1), "+v"(wo0));
    asm volatile("" :: "s"(p.x), "s"(p.wqkv), "s"(p.wo), "s"(p.wr), "s"(p.wg), "s"(p.wu), "s"(p.wd), "s"(p.gw), "s"(p.out), "s"(p.ws));

    const int tokr = 128 * tb + 8 * hp + w;
    u64 ep;
    asm volatile("s_load_dwordx2 %0, %1, 0x0" : "=s"(ep) : "s"(ws + WS_EP) : "memory");
    if (w < 2) dma16(w ? p.gw : p.wr, lane * 16, lds0 + L_RW + w * 1024);
    unsigned pq = 0;
    if (CPF && w == 6 && lane < 5) {
        u64 pc; asm volatile("s_getpc_b64 %0" : "=s"(pc));
        asm volatile("global_load_dword %0, %1, %2" : "=v"(pq) : "v"((jj + 32 * lane) * 128), "s"(pc & ~255ull) : "memory"); }
    u32x4 rx;
    asm volatile("global_load_dwordx4 %0, %1, %2" : "=v"(rx) : "v"(lane * 16), "s"(p.x + (size_t)tokr * 512) : "memory");
    const char* xb = (const char*)p.x + (size_t)tb * 131072;
    const char* wb = (const char*)p.wqkv + hp * 64;
    const int rot = ROT == 0 ? 0 : ROT == 1 ? (2 * (jj & 3) + (jj >> 2)) & 7 : ROT == 2 ? (((jj & 3) + (jj >> 2)) & 1) * 4 : ((jj & 3) + (jj >> 2) * 2 + (jj >> 3)) & 3;
    const int rx4 = ROT == 5 ? rot ^ (((xcd ^ (xcd >> 1)) & 1) << 2) : rot;
    auto chunk = [&](int c) { return ROT >= 4 ? c ^ rx4 : (c + rot) & 7; };
    auto issue = [&](int c, int i) {
        const unsigned s = lds0 + (c % NSLOT) * SLOT; const int cc = chunk(c);
        if (i == 0) dma16(xb + cc * 128, xo0, s + w * 2048);
        if (i == 1) dma16(wb + cc * 196608, wo0, s + 16384 + w * 1024);
        if (i == 2) dma16(xb + cc * 128, xo1, s + w * 2048 + 1024);
    };
    const __amdgpu_buffer_rsrc_t rs_os = __builtin_amdgcn_make_buffer_rsrc(ws + WS_O + (size_t)(4 * tb + kq) * 32768 + head * 1024, 0, 1024, 0x00020000);
#pragma unroll
    for (int i = 0; i < 3; ++i) issue(0, i);
    const int fi = min((int)blockIdx.x, 223); const bool ffn = blockIdx.x < 224;
    if (tid < 32) *(LAS unsigned*)(smem + L_W + 4 * tid) = 0u;
    asm volatile("s_waitcnt vmcnt(3) lgkmcnt(0)\n\ts_barrier" : "+v"(rx), "+s"(ep) : "v"(pq) : "memory");
    const u64 tag = ep + 1;
    unsigned gbits;
    {
            const u32x4 rw = *(const LAS u32x4*)(smem + L_RW + lane * 16), rgw = *(const LAS u32x4*)(smem + L_RW + 1024 + lane * 16);
            float ra = 0.f, ga = 0.f;
#pragma unroll
            for (int j = 0; j < 4; ++j) {
                const float a = bflo(rx[j]), b = bfhi(rx[j]);
                ra = fmaf(a, bflo(rw[j]), ra); ra = fmaf(b, bfhi(rw[j]), ra);
                const unsigned pr = cvtpk(a * bflo(rgw[j]), b * bfhi(rgw[j]));
                ga += bflo(pr); ga += bfhi(pr); }
            ra = wave_sum(ra); ga = wave_sum(ga);
            const unsigned rb = cvtpk(ra, 0.f) & 0xffffu;
            const unsigned key = (((rb & 0x8000u) ? (~rb & 0xffffu) : (rb | 0x8000u)) << 11) | (unsigned)(2047 - tokr);
            gbits = cvtpk(sigmoidf_(bfr(ga)), 0.f) & 0xffffu;
            if (lane == 0) *(LAS unsigned*)(smem + L_RK + 4 * w) = key; }
#pragma unroll
    for (int c = 1; c < 3; ++c) {
#pragma unroll
        for (int i = 0; i < 3; ++i) issue(c, i);
        if (c == 1) { lds_bar();
            if (w == 0) {
                const unsigned kk = wave_maxu(*(const LAS unsigned*)(smem + L_RK + 4 * (lane & 7)));
                if (lane == 0) __hip_atomic_store((GAS u64*)(ws + WS_RG) + blockIdx.x, ((u64)kk << 37) | (tag & ((1ull << 37) - 1)), RLX_AGENT);
            }
        }
    }
    const __amdgpu_buffer_rsrc_t rs_rg = __builtin_amdgcn_make_buffer_rsrc(ws + WS_RG, 0, 2048, 0x00020000);
    STAMP(1);
    volatile LAS unsigned* lw = (volatile LAS unsigned*)(smem + L_W);
    auto arrive = [&](int word) -> unsigned {
        unsigned t = 0; asm volatile("s_waitcnt lgkmcnt(0)" ::: "memory");
        if (lane == 0) t = __hip_atomic_fetch_add((LAS unsigned*)(smem + L_W) + word, 1u, __ATOMIC_RELAXED, __HIP_MEMORY_SCOPE_WORKGROUP);
        return __builtin_amdgcn_readfirstlane(t); };
    auto await = [&](int word, unsigned n) {
        while ((unsigned)__builtin_amdgcn_readfirstlane(lw[word]) < n && spin > 0) { --spin; __builtin_amdgcn_s_sleep(2); }
        asm volatile("" ::: "memory"); };
    const char* kvf = ws + WS_KVF + head * 2048;
    u32x4 kf, vc, qf;
    {   f32x4 aq[2] = {}, ak[2] = {}, av[2] = {};
        const int fsw = (t16 >> 1) & 7;
        const int xa = (32 * kq + t16) * 128 + ((g4 ^ fsw) << 4);
        const int wa = g4 * 512 + ((lane & 15) >> 2) * 64 + ((hh ^ (g4 & 1)) << 5) + (lane & 3) * 8;
        u32x4 fx[8][4], fw[2][4];
        auto frag = [&](int c, int i) {
            const LAS char* s = smem + (c % NSLOT) * SLOT; const int kk = i >> 2, j = i & 3;
            if (j == 1 || j == 2) fx[c][2 * kk + j - 1] = *(const LAS u32x4*)(s + (xa ^ (kk * 64)) + (j - 1) * 2048);
            else fw[c & 1][2 * kk + (j ? 1 : 0)] = trd2(s + 16384 + wa + (j ? 4096 : 0) + kk * 2048, 256); };
        wait_bar<6>();
        STAMP(3); CSTAMP(24);
#pragma unroll
        for (int i = 0; i < 8; ++i) { frag(0, i); if (i < 3) issue(3, i); __builtin_amdgcn_sched_barrier(0); }
#pragma unroll
        for (int c = 0; c < 8; ++c) {
            if (c < 5) vm_bar<6>(); else if (c == 5) vm_bar<5>();
            if (c == 7) {
                vm_bar<4>();
                STAMP(10);
#pragma unroll
                for (int i = 0; i < 8; ++i) frag(7, i); }
            if (c == 1) { STAMP(6); CSTAMP(25); }
            if (c == 2) { STAMP(11); CSTAMP(26); }
            if (c == 3) { STAMP(7); CSTAMP(27); }
            if (c == 5) { STAMP(9); CSTAMP(28); }
            __builtin_amdgcn_sched_barrier(0);
#pragma unroll
            for (int i = 0; i < 8; ++i) {
                const int kk = i >> 2, j = i & 3; const u32x4 xx = fx[c][2 * kk + (j & 1)], ww = fw[c & 1][2 * kk + (j >> 1)];
                if (j < 2) ak[j] = mfma16(ww, xx, ak[j]); else av[j - 2] = mfma16(xx, ww, av[j - 2]);
                if (c < 6 && i < 4) { frag(c + 1, 2 * i); frag(c + 1, 2 * i + 1); }
                if (c < 4 && (i & 1) == 0 && i < 6) issue(c + 4, i >> 1);
                if ((c == 4 || c == 5) && (i & 3) == 1) { const int n = (2 * (c - 4) + (i >> 2)) ^ (rot & 3);
                    dma16(wb + (64 * w + 16 * n) * 3072, (lane >> 2) * 3072 + (((lane & 3) ^ ((lane >> 4) & 2)) << 4), lds0 + L_WQ + (4 * w + n) * 1024); }
                __builtin_amdgcn_sched_barrier(0);
            }
        }
        kf = pack_pair(ak[0], ak[1]); vc = pack_pair(av[0], av[1]);
        asm volatile("s_waitcnt vmcnt(0)" : "+v"(kf), "+v"(vc) :: "memory");
        if (lane == 0) __hip_atomic_fetch_add((LAS unsigned*)(smem + L_W) + W_WQ, 1u, __ATOMIC_RELAXED, __HIP_MEMORY_SCOPE_WORKGROUP);
        __builtin_amdgcn_raw_buffer_store_b128(kf, rs_kv, lane * 16 + tb * 8192, 0, 16);
        __builtin_amdgcn_raw_buffer_store_b128(vc, rs_kv, lane * 16 + tb * 8192 + 1024, 0, 16);
        STAMP(12); CSTAMP(29);
        asm volatile("" ::: "memory");
        await(W_WQ, 8);
        auto publish_kv = [&]() {
            asm volatile("s_waitcnt vmcnt(0)" ::: "memory");
            if (arrive(W_D + hh) == 3 && lane == 0) asm volatile("global_store_dwordx2 %0, %1, %2 sc1" :: "v"(tb * 128), "v"(tag), "s"(kvf) : "memory"); };
        u32x4 fq[8][2];
        auto fragq = [&](int c) { const LAS char* s = smem + L_WQ + wa + chunk(c) * 4096; fq[c][0] = trd2(s, 256); fq[c][1] = trd2(s + 2048, 256); };
        fragq(0); fragq(1); fragq(2);
        __builtin_amdgcn_sched_barrier(0);
#pragma unroll
        for (int c = 0; c < 8; ++c) {
            aq[0] = mfma16(fq[c][0], fx[c][0], aq[0]); aq[1] = mfma16(fq[c][0], fx[c][1], aq[1]);
            if (c + 3 < 8) fragq(c + 3);
            if (c == DPOS && hh == 0) publish_kv();
            if (c == 7 && hh == 1) publish_kv();
            __builtin_amdgcn_sched_barrier(0);
            aq[0] = mfma16(fq[c][1], fx[c][2], aq[0]); aq[1] = mfma16(fq[c][1], fx[c][3], aq[1]);
            __builtin_amdgcn_sched_barrier(0);
        }
        constexpr float QS = 0.25f * 1.4426950408889634f;
        qf = pack_pair(aq[0] * QS, aq[1] * QS);
        *(LAS u32x4*)(smem + L_Q + w * 1024 + lane * 16) = qf;
        __builtin_amdgcn_sched_barrier(0);
    }
    STAMP(4);

    const unsigned pat = ((lane & 11) == 0) ? 0x3f803f80u : 0u;
    u32x4 xv0, xv1;
#pragma unroll
    for (int j = 0; j < 4; ++j) { auto r = __builtin_amdgcn_permlane16_swap(vc[j], pat, false, false); xv0[j] = r[0]; xv1[j] = r[1]; }
    f32x16 o0 = {}, o1 = {}, o2 = {}, o3 = {};
    const int idx = 0xdddd;
    asm volatile("" : "+v"(o0), "+v"(o1), "+v"(o2), "+v"(o3), "+v"(xv0), "+v"(xv1));
    u32x4 rg0, rg1;
    if (w == 5) { rg0 = __builtin_amdgcn_raw_buffer_load_b128(rs_rg, lane * 32, 0, 16); rg1 = __builtin_amdgcn_raw_buffer_load_b128(rs_rg, lane * 32 + 16, 0, 16); }
    arrive(W_Q + hh);
    if (hh == 1) __builtin_amdgcn_raw_buffer_store_b128((u32x4){SENT, SENT, SENT, SENT}, rs_os, lane * 16, 0, 16);
    if (hh == 1) __builtin_amdgcn_s_setprio(1); else __builtin_amdgcn_s_setprio(2);
    await(W_Q + hh, 4);
    STAMP(13);
    u32x4 yk, yv0, yv1, zk, zv0, zv1, uk, uv0, uv1;
    yv0 = yv1 = zv0 = zv1 = uv0 = uv1 = (u32x4){pat, pat, pat, pat};
    yk = zk = uk = yv0;
    const u64 vm = 0x0000ffff0000ffffull;
#define KV9 [yk] "+v"(yk), [yv0] "+v"(yv0), [yv1] "+v"(yv1), [zk] "+v"(zk), [zv0] "+v"(zv0), [zv1] "+v"(zv1), [uk] "+v"(uk), [uv0] "+v"(uv0), [uv1] "+v"(uv1)
    const unsigned need = (15u << (tb & 12)) & ~(1u << tb);
    u64 fl0 =
        __hip_atomic_load((GAS u64*)(kvf + t16 * 128), RLX_AGENT);
    __builtin_amdgcn_sched_barrier(0);
    const u32x4 q0 = qf;
    const u32x4 q1 = *(const LAS u32x4*)(smem + L_Q + (hh * 4 + ((kq + 1) & 3)) * 1024 + lane * 16);
    const u32x4 q2 = *(const LAS u32x4*)(smem + L_Q + (hh * 4 + ((kq + 2) & 3)) * 1024 + lane * 16);
    const u32x4 q3 = *(const LAS u32x4*)(smem + L_Q + (hh * 4 + ((kq + 3) & 3)) * 1024 + lane * 16);
    asm volatile(
        "s_nop 1\n\t" QKM(SA, "%[k]", "%[q0]") "s_nop 11\n\t" EX(192) EX(193)
        UNIT_A(O0, "%[v0]", "%[v1]", QKM(SB, "%[k]", "%[q1]"))
        UNIT_B(O1, "%[v0]", "%[v1]", QKM(SA, "%[k]", "%[q2]"))
        UNIT_A(O2, "%[v0]", "%[v1]", QKM(SB, "%[k]", "%[q3]"))
        UNIT_B_LAST(O3, "%[v0]", "%[v1]", "") "s_nop 11"
        : O_OPS
        : [k] "v"(kf), [v0] "v"(xv0), [v1] "v"(xv1), [q0] "v"(q0), [q1] "v"(q1), [q2] "v"(q2), [q3] "v"(q3), [idx] "v"(idx)
        : S_CLOB);
    asm volatile("" : KV9);
    asm volatile("" : "+v"(fl0));
    const unsigned seen0 = (unsigned)__ballot(fl0 == tag) & 0xffffu;
    if (lane == 0) __hip_atomic_fetch_or((LAS unsigned*)(smem + L_W) + W_KM + hh, seen0, __ATOMIC_RELAXED, __HIP_MEMORY_SCOPE_WORKGROUP);
    STAMP(15);
    if (w == 5) {
        const unsigned tl = (unsigned)tag, th = (unsigned)(tag >> 32) & 31u;
        auto fresh = [&]() { return __all((((rg0[0] ^ tl) | (rg0[2] ^ tl) | (rg1[0] ^ tl) | (rg1[2] ^ tl)) | (((rg0[1] ^ th) | (rg0[3] ^ th) | (rg1[1] ^ th) | (rg1[3] ^ th)) & 31u)) == 0); };
        if (!fresh()) do {
            --spin; asm volatile("" ::: "memory");
            rg0 = __builtin_amdgcn_raw_buffer_load_b128(rs_rg, lane * 32, 0, 16); rg1 = __builtin_amdgcn_raw_buffer_load_b128(rs_rg, lane * 32 + 16, 0, 16);
        } while (!fresh() && spin > 0);
        const unsigned kk = __builtin_amdgcn_readfirstlane(wave_maxu(max(max(rg0[1], rg0[3]), max(rg1[1], rg1[3])) >> 5));
        lw[W_SEL] = kk | 0x80000000u;
        if (blockIdx.x == 0 && lane == 0) __hip_atomic_store((GAS u64*)(ws + WS_EP), tag, RLX_AGENT);
    }
    auto poll_kv = [&](unsigned bits) {
        if ((seen0 & bits) != bits) for (;;) {
            const unsigned m = __builtin_amdgcn_readfirstlane(lw[W_KM + hh]);
            if ((m & bits) == bits || spin <= 0) break;
            --spin;
            if (kq < NPOLL) {
                const unsigned v = (unsigned)__ballot(__hip_atomic_load((GAS u64*)(kvf + t16 * 128), RLX_AGENT) == tag) & 0xffffu;
                if (lane == 0) __hip_atomic_fetch_or((LAS unsigned*)(smem + L_W) + W_KM + hh, v, __ATOMIC_RELAXED, __HIP_MEMORY_SCOPE_WORKGROUP); }
            else __builtin_amdgcn_s_sleep(1); }
        asm volatile("" ::: "memory"); };
    poll_kv(1u << (tb ^ 1));
    { int t;
      asm volatile(LOADB("0x2000", "y") : KV9, [t] "=&s"(t) : [sb] "s"(tb * 8192), [ko] "v"(lane * 16), [rs] "s"(rs_kv), [vm] "s"(vm) : "scc", "memory");
      poll_kv(need);
      asm volatile(LOADB("0x4000", "z") LOADB("0x6000", "u") : KV9, [t] "=&s"(t) : [sb] "s"(tb * 8192), [ko] "v"(lane * 16), [rs] "s"(rs_kv), [vm] "s"(vm) : "scc", "memory"); }
    STAMP(2);
    STAMP(8);
    asm volatile("s_waitcnt vmcnt(6)" : KV9 :: "memory");
    if (hh == 1) arrive(W_PF);
    if (lane == 0) __hip_atomic_store((GAS u64*)(ws + WS_G) + tokr, ((u64)gbits << 48) | (tag & ((1ull << 48) - 1)), RLX_AGENT);
    const int tb3 = 2 * xcd + (jj & 1), cg3 = jj >> 1;
    STAMP(16);
    CSTAMP(30);
    { int gi = 0x8000, cnt = 3, sb = tb * 8192, t, u;
      int rdy = 0, m0s; u64 fl;
      unsigned pa[3], pb[3];
#pragma unroll
      for (int j = 1; j < 4; ++j) { const unsigned d = lds0 + L_CB + ((hh * 4 + ((kq + j) & 3)) * 3 + (j - 1)) * 2304; pa[j - 1] = d + lane * 16; pb[j - 1] = d + 2048 + lane * 4; }
      spin = __builtin_amdgcn_readfirstlane(spin);
      int wi = WROT * (jj & 3) * 0x8000;
      const __amdgpu_buffer_rsrc_t rs_wo = __builtin_amdgcn_make_buffer_rsrc((char*)p.wo + cg3 * 64, 0, 524288 - cg3 * 64, 0x00020000);
      asm volatile(
          "global_load_dwordx2 %[fl], %[fo], %[fb] sc1\n\t"
          "s_mov_b32 %[m0s], m0\n\ts_setprio 0\n\t" QKM(SA, "%[yk]", "%[q1]") "s_nop 11\n\t" EX(192) EX(193)
          "1:\n\t"
          TILE2("y", "z", "s_cmp_eq_u32 %[cnt], 3\n\ts_cbranch_scc1 6f\n\t" GROUP LOADB("0", "x")
                "s_cmp_lg_u32 %[cnt], 0\n\ts_cbranch_scc1 6f\n\tds_write_b32 %[pre], %[idx]\n\t6:\n\t",
                "s_cmp_lg_u32 %[cnt], 3\n\ts_cbranch_scc1 6f\n\t" GROUP LOADB("0", "x") "6:\n\t")
          TILE3("z", "u", WODMA LOADB("0x2000", "y"), WAITX("%[xa]"), "")
          "s_sub_u32 %[cnt], %[cnt], 1\n\ts_cbranch_scc1 4f\n\t"
          TILE3("u", "x", LOADB("0x4000", "z"), WAITX("%[xb]"), "")
          TILE("x", "y", LOADB("0x6000", "u"))
          "s_branch 1b\n\t"
          "4:\n\t"
          UNIT_A(O1, "%[uv0]", "%[uv1]", QKM(SB, "%[uk]", "%[q2]"))
          UNIT_BX(O2, "%[uv0]", "%[uv1]", QKM(SA, "%[uk]", "%[q3]"), PUT(1, "v[144:147]", "v[148:151]", "v152"))
          UNIT_AX(O3, "%[uv0]", "%[uv1]", QKM(SB, "%[uk]", "%[q0]"), PUT(2, "v[160:163]", "v[164:167]", "v168"))
          UNIT_B_LAST(O0, "%[uv0]", "%[uv1]", PUT(3, "v[176:179]", "v[180:183]", "v184"))
          "s_mov_b32 m0, %[m0s]\n\ts_setprio 1\n\ts_waitcnt vmcnt(0)\n\ts_nop 11"
          : O_OPS,
            [xk] "+v"(kf), [xv0] "+v"(xv0), [xv1] "+v"(xv1), [yk] "+v"(yk), [yv0] "+v"(yv0), [yv1] "+v"(yv1),
            [zk] "+v"(zk), [zv0] "+v"(zv0), [zv1] "+v"(zv1), [uk] "+v"(uk), [uv0] "+v"(uv0), [uv1] "+v"(uv1),
            [gi] "+s"(gi), [cnt] "+s"(cnt), [rdy] "+s"(rdy), [spin] "+s"(spin),
            [sb] "+s"(sb), [t] "=&s"(t), [u] "=&s"(u), [fl] "=&v"(fl), [m0s] "=&s"(m0s), [wi] "+s"(wi)
          : [q0] "v"(q0), [q1] "v"(q1), [q2] "v"(q2), [q3] "v"(q3), [idx] "v"(idx), [ko] "v"(lane * 16),
            [rs] "s"(rs_kv), [vm] "s"(vm), [fo] "v"(t16 * 128), [fb] "s"(kvf), [tag] "s"(tag),
            [rw] "s"(rs_wo), [wl] "s"(lds0 + L_WO + kq * 8192), [wv] "v"((128 * kq + (lane >> 2)) * 1024 + (((lane & 3) ^ ((lane >> 3) & 2)) << 4)), [hh] "s"(hh), [xa] "s"(hh ? 9 : FR), [xb] "s"(hh ? 9 : FR - 1), [fw] "s"(lds0 + L_FW + kq * 2048),
            [wgp] "s"(p.wg + (size_t)(64 * (fi & 7) + 16 * kq) * 1792 + 64 * (fi >> 3)), [wup] "s"(p.wu + (size_t)(64 * (fi & 7) + 16 * kq) * 1792 + 64 * (fi >> 3)),
            [wdp] "s"(p.wd + (size_t)(8 * fi + 2 * kq) * 512), [k1] "v"(lane * 16 + 1024), [pre] "v"(lds0 + L_W + 4 * (W_PRE + kq) * hh + 4 * 15 * (1 - hh)),
            [f0] "v"((lane >> 3) * 3584 + (lane & 7) * 16), [f1] "v"((lane >> 3) * 3584 + (lane & 7) * 16 + 28672),
            [pa1] "v"(pa[0]), [pa2] "v"(pa[1]), [pa3] "v"(pa[2]), [pb1] "v"(pb[0]), [pb2] "v"(pb[1]), [pb3] "v"(pb[2])
          : S_CLOB, "vcc", "scc", "memory"); }
    __builtin_amdgcn_s_waitcnt(0x0f70);
    STAMP(17);
    CSTAMP(31);
    arrive(W_C + hh);
    u32x4 xs0, xs1;
    if (hh == 0) {
        await(W_SEL, 1u);
        const uint16_t* xr = p.x + (size_t)(2047 - (int)(__builtin_amdgcn_readfirstlane(lw[W_SEL]) & 2047u)) * 512 + 64 * (fi & 7) + 8 * g4;
        xs0 = *(const u32x4*)xr; xs1 = *(const u32x4*)(xr + 32); }
    if (hh == 1) { __builtin_amdgcn_s_setprio(3); lw[W_NEAR + kq] = 1u; }
    const int tok = 128 * tb3 + 32 * kq + 16 * hh + t16;
    const GAS u64* gp = (const GAS u64*)(ws + WS_G) + tok;
    u64 gg = __hip_atomic_load(gp, RLX_AGENT);
    const u32x2 xa = *(const u32x2*)(p.x + (size_t)tok * 512 + 32 * cg3 + 4 * g4), xc = *(const u32x2*)(p.x + (size_t)tok * 512 + 32 * cg3 + 16 + 4 * g4);
    await(W_C + hh, 4);
    STAMP(18);
    if (hh == 0) {
        const LAS char* a = smem + L_FW + (8 * g4 + (t16 >> 2)) * 128 + kq * 32 + (lane & 3) * 8;
        f32x4 dg = mfma16(xs0, trd2(a, 512), (f32x4){}), du = mfma16(xs0, trd2(a + 8192, 512), (f32x4){});
        dg = mfma16(xs1, trd2(a + 4096, 512), dg); du = mfma16(xs1, trd2(a + 8192 + 4096, 512), du);
        if (ffn && lane < 32)
            __hip_atomic_store((GAS u64*)(ws + WS_P1) + fi * 128 + g4 * 64 + 16 * kq + t16, ((u64)(unsigned)tag << 32) | __float_as_uint(g4 ? du[0] : dg[0]), RLX_AGENT);
    }
    float o[9];
#pragma unroll
    for (int r = 0; r < 9; ++r) o[r] = o0[r];
#pragma unroll
    for (int j = 0; j < 3; ++j) {
        const LAS char* d = smem + L_CB + (w * 3 + j) * 2304;
        const f32x4 a = *(const LAS f32x4*)(d + lane * 16), b = *(const LAS f32x4*)(d + 1024 + lane * 16);
#pragma unroll
        for (int r = 0; r < 4; ++r) { o[r] += a[r]; o[4 + r] += b[r]; }
        o[8] += *(const LAS float*)(d + 2048 + lane * 4);
    }
    { float mag = 0.f;
#pragma unroll
      for (int r = 0; r < 8; ++r) mag += fabsf(o[r]);
      const bool safe = o[8] >= 9.313225746154785e-10f && o[8] <= 1.8446744073709552e19f && mag <= 3.0e38f;
      if (!__all(safe)) {
          f32x16 acc = {}; float m = -INFINITY;
          u32x4 v0 = {pat, pat, pat, pat}, v1 = v0;
          const __amdgpu_buffer_rsrc_t rs_all = __builtin_amdgcn_make_buffer_rsrc(ws + WS_KV + (size_t)head * 131072, 0, 131072, 0x00020000);
          for (int t = 0; t < 64; ++t) {
              const u32x4 kk = __builtin_amdgcn_raw_buffer_load_b128(rs_all, lane * 16, t * 2048, 16);
              if ((lane & 16) == 0) { v0 = __builtin_amdgcn_raw_buffer_load_b128(rs_all, lane * 16 + 1024, t * 2048, 16); v1 = __builtin_amdgcn_raw_buffer_load_b128(rs_all, lane * 16 + 1280, t * 2048, 16); }
              f32x16 s = __builtin_amdgcn_mfma_f32_32x32x16_bf16(__builtin_bit_cast(bf16x8, kk), __builtin_bit_cast(bf16x8, q0), (f32x16){}, 0, 0, 0);
              float tm = s[0];
#pragma unroll
              for (int r = 1; r < 16; ++r) tm = fmaxf(tm, s[r]);
              auto sw = __builtin_amdgcn_permlane32_swap(__float_as_uint(tm), __float_as_uint(tm), false, false);
              const float mn = fmaxf(m, fmaxf(__uint_as_float(sw[0]), __uint_as_float(sw[1])));
              const float al = __builtin_amdgcn_exp2f(m - mn); m = mn;
#pragma unroll
              for (int r = 0; r < 16; ++r) { s[r] = __builtin_amdgcn_exp2f(s[r] - mn); acc[r] *= al; }
              acc = __builtin_amdgcn_smfmac_f32_32x32x32_bf16(__builtin_bit_cast(bf16x8, v0), __builtin_bit_cast(bf16x16, __builtin_shufflevector(s, s, 0, 1, 2, 3, 4, 5, 6, 7)), acc, idx, 0, 0);
              acc = __builtin_amdgcn_smfmac_f32_32x32x32_bf16(__builtin_bit_cast(bf16x8, v1), __builtin_bit_cast(bf16x16, __builtin_shufflevector(s, s, 8, 9, 10, 11, 12, 13, 14, 15)), acc, idx, 0, 0);
          }
#pragma unroll
          for (int r = 0; r < 9; ++r) o[r] = acc[r];
      } }
    { const float inv = __builtin_amdgcn_rcpf(o[8]);
      const u32x4 of = {cvtpk(o[0] * inv, o[1] * inv), cvtpk(o[2] * inv, o[3] * inv), cvtpk(o[4] * inv, o[5] * inv), cvtpk(o[6] * inv, o[7] * inv)};
      __builtin_amdgcn_raw_buffer_store_b128(unsent(of), rs_os, lane * 16, 0, 16); }
    __builtin_amdgcn_sched_barrier(0);
    if (__any((gg ^ tag) << 16 != 0)) do { --spin; gg = __hip_atomic_load(gp, RLX_AGENT); } while (__any((gg ^ tag) << 16 != 0) && spin > 0);
    asm volatile("" :: "v"(xa), "v"(xc));

    const __amdgpu_buffer_rsrc_t rs_o = __builtin_amdgcn_make_buffer_rsrc(ws + WS_O + (size_t)(4 * tb3 + kq) * 32768, 0, 32768, 0x00020000);
    const int oo = (g4 >> 1) * 2048 + ((g4 & 1) * 32 + t16) * 16;
    const LAS char* wa = smem + L_WO + ((g4 >> 1) * 32 + (g4 & 1) * 4 + ((lane & 15) >> 2)) * 64 + (lane & 3) * 8;
    const int h0 = (g4 & 1) << 5, h1 = h0 ^ 32;
    await(W_SEL, 1u);
    const unsigned skey = __builtin_amdgcn_readfirstlane(lw[W_SEL]);
    const int sel = 2047 - (int)(skey & 2047u);
    const bool own = (sel >> 7) == tb3;
    u32x4 yp[14];
    auto yload = [&]() {
        const __amdgpu_buffer_rsrc_t rs_y = __builtin_amdgcn_make_buffer_rsrc(ws + WS_YP + (size_t)kq * 56 * 4096, 0, 56 * 4096, 0x00020000);
        asm volatile("" ::: "memory");
#pragma unroll
        for (int i = 0; i < 14; ++i) yp[i] = __builtin_amdgcn_raw_buffer_load_b128(rs_y, g4 * 4096 + cg3 * 256 + t16 * 16, i * 16384, 16);
        __builtin_amdgcn_sched_barrier(0); };
    auto ysum = [&]() -> bool {
        float s0 = 0.f, s1 = 0.f; unsigned bad = 0;
#pragma unroll
        for (int i = 0; i < 14; ++i) { s0 += __uint_as_float(yp[i][0]); s1 += __uint_as_float(yp[i][2]); bad |= (yp[i][1] ^ (unsigned)tag) | (yp[i][3] ^ (unsigned)tag); }
        if (!__all(bad == 0) && --spin > 0) return false;
        auto a = __builtin_amdgcn_permlane16_swap(__float_as_uint(s0), __float_as_uint(s1), false, false);
        const float v = __uint_as_float(a[0]) + __uint_as_float(a[1]);
        auto b = __builtin_amdgcn_permlane32_swap(__float_as_uint(v), __float_as_uint(v), false, false);
        if (lane < 32) *(LAS float*)(smem + L_YS + kq * 128 + t16 * 8 + g4 * 4) = __uint_as_float(b[0]) + __uint_as_float(b[1]);
        arrive(W_Y);
        return true; };
    bool ydone = true;
    u32x4 eb[8];
    auto load_even = [&]() {
#pragma unroll
        for (int j = 0; j < 8; ++j) eb[j] = __builtin_amdgcn_raw_buffer_load_b128(rs_o, oo + hh * 256, j * 4096, OAUXL);
        __builtin_amdgcn_sched_barrier(0); };
    if (hh == 0) {
        asm volatile("s_waitcnt vmcnt(0)" ::: "memory");
        if (arrive(W_O) == 3) {
            await(W_PF, 4);
            if (lane == 0) __hip_atomic_store((GAS u64*)(ws + WS_OF + (tb * 16 + hp) * 128), tag, RLX_AGENT); }
        if (ffn) {
            const __amdgpu_buffer_rsrc_t rs_p = __builtin_amdgcn_make_buffer_rsrc(ws + WS_P1 + (size_t)(fi >> 3) * 8192, 0, 8192, 0x00020000);
            const int po = (lane >> 3) * 1024 + ((lane >> 2) & 1) * 512 + (fi & 7) * 64 + (lane & 3) * 16;
            u32x4 pv = __builtin_amdgcn_raw_buffer_load_b128(rs_p, po, 0, 16);
            while (!__all(pv[1] == (unsigned)tag && pv[3] == (unsigned)tag) && spin > 0) {
                --spin; __builtin_amdgcn_s_sleep(4); asm volatile("" ::: "memory"); pv = __builtin_amdgcn_raw_buffer_load_b128(rs_p, po, 0, 16); }
            auto s32 = __builtin_amdgcn_permlane32_swap(pv[0], pv[2], false, false);
            float v = __uint_as_float(s32[0]) + __uint_as_float(s32[1]);
            auto s16 = __builtin_amdgcn_permlane16_swap(__float_as_uint(v), __float_as_uint(v), false, false);
            v = __uint_as_float(s16[0]) + __uint_as_float(s16[1]); v += dppf<0x128>(v);
            const float gs = bfr(v), us = dppf<0x124>(gs);
            const float hv = bfr(gs * sigmoidf_(gs)) * us;
            auto hs = __builtin_amdgcn_permlane32_swap(__float_as_uint(hv), __float_as_uint(hv), false, false);
            const unsigned hp2 = cvtpk(__uint_as_float(hs[0]), __uint_as_float(hs[1]));
            unsigned wd2[8];
#pragma unroll
            for (int f = 0; f < 8; ++f) wd2[f] = *(const LAS unsigned*)(smem + L_WD + f * 1024 + kq * 256 + lane * 4);
            float y0 = 0.f, y1 = 0.f;
#pragma unroll
            for (int f = 0; f < 4; ++f) {
                const unsigned h = __builtin_amdgcn_readlane(hp2, f);
                y0 = dot2(lo2(wd2[2 * f], wd2[2 * f + 1]), h, y0); y1 = dot2(hi2(wd2[2 * f], wd2[2 * f + 1]), h, y1); }
            const __amdgpu_buffer_rsrc_t rs_w = __builtin_amdgcn_make_buffer_rsrc(ws + WS_YP + (size_t)fi * 4096, 0, 4096, 0x00020000);
            __builtin_amdgcn_raw_buffer_store_b128((u32x4){__float_as_uint(y0), (unsigned)tag, __float_as_uint(y1), (unsigned)tag}, rs_w, lane * 16 + kq * 1024, 0, 16);
        }
        for (;;) {
            unsigned m;
            if (kq == 3) { m = (unsigned)__ballot(__hip_atomic_load((GAS u64*)(ws + WS_OF + (tb3 * 16 + t16) * 128), RLX_AGENT) == tag); lw[W_EM] = m; }
            else m = __builtin_amdgcn_readfirstlane(lw[W_EM]);
            if (m == ~0u || spin <= 0) break;
            --spin; __builtin_amdgcn_s_sleep(8); }
        asm volatile("" ::: "memory");
        load_even();
        if (own) {
            while ((unsigned)__builtin_amdgcn_readfirstlane(lw[W_PRE + kq]) == 0 && spin > 0) { --spin; __builtin_amdgcn_s_sleep(6); }
            yload(); }
        while ((unsigned)__builtin_amdgcn_readfirstlane(lw[W_NEAR + kq]) == 0 && spin > 0) { --spin; __builtin_amdgcn_s_sleep(6); }
        asm volatile("" ::: "memory");
        if (own) ydone = ysum();
        __builtin_amdgcn_s_waitcnt(0x0f70);
    } else {
        await(W_EM, ~0u);
        asm volatile("" ::: "memory");
        load_even();
    }
    u32x4 wf[16];
#pragma unroll
    for (int j = 0; j < 8; ++j) { wf[2 * j] = trd2(wa + 1024 + j * 4096 + h0, 512); wf[2 * j + 1] = trd2(wa + 1024 + j * 4096 + h1, 512); }
    const float gt = bfhi((unsigned)(gg >> 32));
    float bb[8] = {bflo(xa[0]), bfhi(xa[0]), bflo(xa[1]), bfhi(xa[1]), bflo(xc[0]), bfhi(xc[0]), bflo(xc[1]), bfhi(xc[1])};
    const float gm = bfr(1.0f - gt);
#pragma unroll
    for (int r = 0; r < 8; ++r) { bb[r] = bfr(gm * bb[r]); asm volatile("" : "+v"(bb[r])); }
#pragma unroll
    for (int j = 0; j < 16; ++j) asm volatile("" : "+v"(wf[j]));
    if (hh == 1) { asm volatile("s_waitcnt vmcnt(8)" ::: "memory"); lw[W_GO + kq] = 1u; }
    else {
        while ((unsigned)__builtin_amdgcn_readfirstlane(lw[W_GO + kq]) == 0 && spin > 0) --spin;
        __builtin_amdgcn_s_setprio(GOPRIO); asm volatile("" ::: "memory"); }
    STAMP(19);
    u32x4 ob[8];
    auto load_odd = [&](int from) {
#pragma unroll
        for (int j = 0; j < 8; ++j) if (j >= from) { ob[j] = __builtin_amdgcn_raw_buffer_load_b128(rs_o, oo + 1024 + hh * 256, j * 4096, OAUXL); __builtin_amdgcn_sched_barrier(0); } };
    load_odd(0);
    f32x4 c0 = {}, c1 = {};
#pragma unroll
    for (int j = 0; j < 8; ++j) { c0 = mfma16(trd2(wa + j * 4096 + h0, 512), eb[j], c0); c1 = mfma16(trd2(wa + j * 4096 + h1, 512), eb[j], c1); }
    asm volatile("" : "+v"(c0), "+v"(c1));
    __builtin_amdgcn_sched_barrier(0);
#pragma unroll
    for (int g = 0; g < 4; ++g) {
        auto missing = [&]() { const u32x4 a = ob[2 * g], b = ob[2 * g + 1];
            return __any(max(max4(a), max4(b)) == SENT); };
        if (missing()) do { --spin; asm volatile("" ::: "memory"); load_odd(2 * g); } while (missing() && spin > 0);
        c0 = mfma16(wf[4 * g], ob[2 * g], c0); c1 = mfma16(wf[4 * g + 1], ob[2 * g], c1);
        c0 = mfma16(wf[4 * g + 2], ob[2 * g + 1], c0); c1 = mfma16(wf[4 * g + 3], ob[2 * g + 1], c1);
    }
    STAMP(20);
    if (own) {
        while (!ydone) { yload(); ydone = ysum(); }
        if (((sel >> 4) & 7) == 2 * kq + hh) await(W_Y, 4); }
    if (own && tok == sel) {
        const float xo[8] = {bflo(xa[0]), bfhi(xa[0]), bflo(xa[1]), bfhi(xa[1]), bflo(xc[0]), bfhi(xc[0]), bflo(xc[1]), bfhi(xc[1])};
        const unsigned mono = (skey >> 11) & 0xffffu, rb = (mono & 0x8000u) ? (mono & 0x7fffu) : (~mono & 0xffffu);
        const float rsel = bfr(sigmoidf_(bflo(rb)));
        f32x4 y0 = {}, y1 = {};
#pragma unroll
        for (int i = 0; i < 4; ++i) { y0 += *(const LAS f32x4*)(smem + L_YS + i * 128 + g4 * 16); y1 += *(const LAS f32x4*)(smem + L_YS + i * 128 + 64 + g4 * 16); }
#pragma unroll
        for (int r = 0; r < 4; ++r) { bb[r] = bfr(gm * bfr(xo[r] + bfr(bfr(y0[r]) * rsel))); bb[4 + r] = bfr(gm * bfr(xo[4 + r] + bfr(bfr(y1[r]) * rsel))); }
    }
    f32x4 r0, r1;
#pragma unroll
    for (int r = 0; r < 4; r += 2) {
        const unsigned a0 = cvtpk(c0[r], c0[r + 1]), a1 = cvtpk(c1[r], c1[r + 1]);
        const unsigned t0 = cvtpk(gt * bflo(a0), gt * bfhi(a0)), t1 = cvtpk(gt * bflo(a1), gt * bfhi(a1));
        r0[r] = bflo(t0) + bb[r]; r0[r + 1] = bfhi(t0) + bb[r + 1]; r1[r] = bflo(t1) + bb[4 + r]; r1[r + 1] = bfhi(t1) + bb[5 + r]; }
    { const __amdgpu_buffer_rsrc_t rs_out = __builtin_amdgcn_make_buffer_rsrc(p.out, 0, 2u << 20, 0x00020000);
      __builtin_amdgcn_raw_buffer_store_b128(pack_pair(r0, r1), rs_out, tok * 1024 + 64 * cg3 + 32 * (g4 & 1) + 16 * (g4 >> 1), 0, 0); }
}

extern "C" void kernel_launch(void* const* d_in, const int* in_sizes, int n_in,
                              void* d_out, int out_size, void* d_ws, size_t ws_size,
                              hipStream_t stream) {
    Params p;
    p.x = (const uint16_t*)d_in[0]; p.wqkv = (const uint16_t*)d_in[1]; p.wo = (const uint16_t*)d_in[2];
    p.wr = (const uint16_t*)d_in[4]; p.wg = (const uint16_t*)d_in[5]; p.wu = (const uint16_t*)d_in[6];
    p.wd = (const uint16_t*)d_in[7]; p.gw = (const uint16_t*)d_in[8];
    p.out = (uint16_t*)d_out; p.ws = (char*)d_ws;
    mix_kernel<<<256, 512, 0, stream>>>(p);
}
```
